```python
import torch

def _softmax(x, axis=-1):
    return torch.softmax(x, dim=axis)

def _gelu(x):
    return torch.nn.functional.gelu(x, approximate='tanh')

def _silu(x):
    return torch.nn.functional.silu(x)

def _sigmoid(x):
    return torch.sigmoid(x)

def _layernorm(x, axis=-1, eps=1e-05):
    m = x.mean(dim=axis, keepdim=True)
    v = x.var(dim=axis, keepdim=True, unbiased=False)
    return (x - m) / torch.sqrt(v + eps)

def _scatter_add(tgt, idx, src, axis=0):
    return tgt.clone().index_add_(axis, idx, src)

def _segment_sum(src, seg, n):
    out = torch.zeros((n,) + src.shape[1:], dtype=src.dtype)
    return out.index_add_(0, seg, src)

def _pad(x, pw):
    flat = tuple((v for pair in reversed(pw) for v in pair))
    return torch.nn.functional.pad(x, flat)
kernel_name = 'parallel_sum__linear_attn_block__mixer_gmlp__prompt'

def setup_inputs():

    _w = lambda a: (lambda t: t.to(torch.bfloat16) if t.is_floating_point() else t)(torch.as_tensor(a))

    def _A_setup():
        B, S, D = (1, 2048, 640)
        return {'x': _w((lambda *s: torch.randn(*s, dtype=torch.bfloat16) * 0.05)(B, S, D)), 'wqkv': _w((lambda *s: torch.randn(*s, dtype=torch.bfloat16) * 0.8)(D, 3 * D)), 'wo': _w((lambda *s: torch.randn(*s, dtype=torch.bfloat16) * 0.2)(D, D))}
    _A_in = _A_setup()

    def _B_setup():
        B, S, D, F = (1, 2048, 640, 1728)
        Fh = F // 2
        return {'x': _w((lambda *s: torch.randn(*s, dtype=torch.bfloat16) * 0.05)(B, S, D)), 'w_in': _w((lambda *s: torch.randn(*s, dtype=torch.bfloat16) * 0.4)(D, 2 * Fh)), 'w_sp': _w(0.01 * (lambda *s: torch.randn(*s, dtype=torch.bfloat16) * 0.4)(S, S)), 'b_sp': _w(1.0 + 0.01 * (lambda *s: torch.randn(*s, dtype=torch.bfloat16) * 0.05)(S)), 'w_out': _w((lambda *s: torch.randn(*s, dtype=torch.bfloat16) * 0.05)(Fh, D))}
    _B_in = _B_setup()
    return {'A_x': _A_in['x'], 'A_wqkv': _A_in['wqkv'], 'A_wo': _A_in['wo'], 'B_x': _B_in['x'], 'B_w_in': _B_in['w_in'], 'B_w_sp': _B_in['w_sp'], 'B_b_sp': _B_in['b_sp'], 'B_w_out': _B_in['w_out']}

def reference(A_x, A_wqkv, A_wo, B_x, B_w_in, B_w_sp, B_b_sp, B_w_out):

    def _A_ref(x, wqkv, wo):
        B, S, D = x.shape
        H = 32
        Dh = D // H
        qkv = (lambda x, *d: x.permute(*d))((x @ wqkv).reshape(B, S, 3, H, Dh), 2, 0, 3, 1, 4)
        q, k, v = (qkv[0], qkv[1], qkv[2])
        phi = lambda t: torch.where(t > 0, t + 1, torch.exp(t)) + 1e-06
        pq, pk = (phi(q), phi(k))
        kv = torch.einsum('bhsd,bhse->bhde', pk, v)
        z = torch.einsum('bhsd->bhd', pk)
        num = torch.einsum('bhqd,bhde->bhqe', pq, kv)
        den = torch.einsum('bhqd,bhd->bhq', pq, z)[..., None] + 1e-06
        o = num / den
        return (lambda x, *d: x.permute(*d))(o, 0, 2, 1, 3).reshape(B, S, D) @ wo

    def _B_ref(x, w_in, w_sp, b_sp, w_out):
        B, S, D = x.shape
        F = w_in.shape[-1]
        Fh = F // 2
        z = _gelu(torch.einsum('bsd,df->bsf', x, w_in))
        z1 = z[..., :Fh].clone()
        z2 = _layernorm(z[..., Fh:], axis=-1)
        g = torch.einsum('ts,bsf->btf', w_sp, z2) + b_sp[None, :, None]
        return torch.einsum('bsf,fd->bsd', z1 * g, w_out)
    x = A_x
    xn = _layernorm(x, axis=-1)
    return x + _A_ref(xn, A_wqkv, A_wo) + _B_ref(xn, B_w_in, B_w_sp, B_b_sp, B_w_out)
```

```cpp
#include <hip/hip_runtime.h>
#include <hip/hip_bf16.h>
typedef unsigned short u16;
__device__ __forceinline__ float bf2f(u16 b){ return __uint_as_float(((unsigned)b)<<16); }
__device__ __forceinline__ u16 f2bf(float f){ unsigned u=__float_as_uint(f); u += 0x7fffu + ((u>>16)&1u); return (u16)(u>>16); }
constexpr int S=2048, D=640, H=32, DH=20, F=1728, FH=864;

__global__ void ln_k(const u16* x, int ld, int off, int n, u16* y, int ldy){
  __shared__ float red[256];
  int r=blockIdx.x, t=threadIdx.x;
  float s=0; for(int i=t;i<n;i+=256) s+=bf2f(x[(size_t)r*ld+off+i]);
  red[t]=s; __syncthreads(); for(int o=128;o>0;o>>=1){ if(t<o) red[t]+=red[t+o]; __syncthreads(); }
  float m=red[0]/n; __syncthreads();
  float v=0; for(int i=t;i<n;i+=256){ float d=bf2f(x[(size_t)r*ld+off+i])-m; v+=d*d; }
  red[t]=v; __syncthreads(); for(int o=128;o>0;o>>=1){ if(t<o) red[t]+=red[t+o]; __syncthreads(); }
  float rs=rsqrtf(red[0]/n+1e-5f);
  for(int i=t;i<n;i+=256) y[(size_t)r*ldy+i]=f2bf((bf2f(x[(size_t)r*ld+off+i])-m)*rs);
}
template<int MODE> __global__ void gemm_k(const u16* A, int lda, const u16* B, int ldb, int K, u16* C, int ldc, float* Cf, const u16* e1, int lde1, const u16* e2){
  __shared__ float As[16][17], Bs[16][17];
  int tx=threadIdx.x, ty=threadIdx.y; int row=blockIdx.y*16+ty, col=blockIdx.x*16+tx;
  float acc=0;
  for(int k0=0;k0<K;k0+=16){
    As[ty][tx]=bf2f(A[(size_t)row*lda+k0+tx]); Bs[ty][tx]=bf2f(B[(size_t)(k0+ty)*ldb+col]);
    __syncthreads();
    for(int k=0;k<16;k++) acc+=As[ty][k]*Bs[k][tx];
    __syncthreads();
  }
  if(MODE==0){
    float v=bf2f(f2bf(acc));
    if(col<2*D){ v = (v>0? v+1.f : __expf(v)) + 1e-6f; }
    C[(size_t)row*ldc+col]=f2bf(v);
  } else if(MODE==1){
    float v=bf2f(f2bf(acc));
    float u=0.7978845608f*(v+0.044715f*v*v*v);
    C[(size_t)row*ldc+col]=f2bf(0.5f*v*(1.f+tanhf(u)));
  } else if(MODE==2){
    float g=bf2f(f2bf(bf2f(f2bf(acc))+bf2f(e2[row])));
    C[(size_t)row*ldc+col]=f2bf(bf2f(e1[(size_t)row*lde1+col])*g);
  } else if(MODE==3){ Cf[(size_t)row*ldc+col]=acc; }
  else if(MODE==4){
    C[(size_t)row*ldc+col]=f2bf(bf2f(e1[(size_t)row*lde1+col])+Cf[(size_t)row*ldc+col]+acc);
  }
}
__global__ void kv_k(const u16* qkv, float* kv, float* z){
  int h=blockIdx.x, t=threadIdx.x;
  if(t<400){ int d=t/20, e=t%20; float a=0; for(int s=0;s<S;s++) a+=bf2f(qkv[(size_t)s*3*D+D+h*20+d])*bf2f(qkv[(size_t)s*3*D+2*D+h*20+e]); kv[h*400+t]=a; }
  else if(t<420){ int d=t-400; float a=0; for(int s=0;s<S;s++) a+=bf2f(qkv[(size_t)s*3*D+D+h*20+d]); z[h*20+d]=a; }
}
__global__ void o_k(const u16* qkv, const float* kv, const float* z, u16* o){
  int s=blockIdx.x, c=threadIdx.x; int h=c/20, e=c%20;
  float num=0, den=0;
  for(int d=0;d<20;d++){ float p=bf2f(qkv[(size_t)s*3*D+h*20+d]); num+=p*kv[h*400+d*20+e]; den+=p*z[h*20+d]; }
  o[(size_t)s*D+c]=f2bf(num/(den+1e-6f));
}
extern "C" void kernel_launch(void* const* d_in, const int* in_sizes, int n_in, void* d_out, int out_size, void* d_ws, size_t ws_size, hipStream_t stream){
  const u16* x=(const u16*)d_in[0]; const u16* wqkv=(const u16*)d_in[1]; const u16* wo=(const u16*)d_in[2];
  const u16* w_in=(const u16*)d_in[4]; const u16* w_sp=(const u16*)d_in[5]; const u16* b_sp=(const u16*)d_in[6]; const u16* w_out=(const u16*)d_in[7];
  char* w=(char*)d_ws;
  u16* xn=(u16*)w; w+=S*D*2; u16* qkv=(u16*)w; w+=S*3*D*2; u16* z=(u16*)w; w+=S*F*2; u16* z2n=(u16*)w; w+=S*FH*2; u16* y=(u16*)w; w+=S*FH*2; u16* o=(u16*)w; w+=S*D*2;
  float* kv=(float*)w; w+=32*400*4; float* zz=(float*)w; w+=32*20*4+512; float* cf=(float*)w;
  dim3 b(16,16);
  ln_k<<<S,256,0,stream>>>(x,D,0,D,xn,D);
  gemm_k<0><<<dim3(3*D/16,S/16),b,0,stream>>>(xn,D,wqkv,3*D,D,qkv,3*D,nullptr,nullptr,0,nullptr);
  gemm_k<1><<<dim3(F/16,S/16),b,0,stream>>>(xn,D,w_in,F,D,z,F,nullptr,nullptr,0,nullptr);
  ln_k<<<S,256,0,stream>>>(z,F,FH,FH,z2n,FH);
  gemm_k<2><<<dim3(FH/16,S/16),b,0,stream>>>(w_sp,S,z2n,FH,S,y,FH,nullptr,z,F,b_sp);
  kv_k<<<32,448,0,stream>>>(qkv,kv,zz);
  o_k<<<S,640,0,stream>>>(qkv,kv,zz,o);
  gemm_k<3><<<dim3(D/16,S/16),b,0,stream>>>(o,D,wo,D,D,nullptr,D,cf,nullptr,0,nullptr);
  gemm_k<4><<<dim3(D/16,S/16),b,0,stream>>>(y,FH,w_out,D,FH,(u16*)d_out,D,cf,x,D,nullptr);
}
```

```cpp
#include <hip/hip_runtime.h>
#include <hip/hip_bf16.h>
#include <utility>

typedef unsigned int u32;
typedef unsigned long long u64;
typedef unsigned char u8;
typedef u32 v4u __attribute__((ext_vector_type(4)));
typedef u32 v2u __attribute__((ext_vector_type(2)));
typedef float f16v __attribute__((ext_vector_type(16)));
typedef float f4v __attribute__((ext_vector_type(4)));
typedef float f2v __attribute__((ext_vector_type(2)));
typedef short s8v __attribute__((ext_vector_type(8)));
typedef short s4v __attribute__((ext_vector_type(4)));
typedef __bf16 b2v __attribute__((ext_vector_type(2)));
#pragma clang diagnostic ignored "-Wint-to-pointer-cast"
#define LAS __attribute__((address_space(3)))
#define DI __device__ __forceinline__
#define SB() __builtin_amdgcn_sched_barrier(0)
#define STAMPS 0

constexpr u32 OFF_FLAG = 4096;
constexpr u32 OFF_DBG = 8192;
constexpr u32 OFF_STATS = 0x20000;
constexpr u32 OFF_Z2T = 0x60000;
constexpr u32 OFF_PKV = OFF_Z2T + 54 * 64 * 1024;
constexpr u32 OFF_PQ = OFF_PKV + 32 * 80 * 2 * 1024;
constexpr u32 OFF_Z1 = OFF_PQ + 32 * 40 * 4 * 512;
constexpr u32 OFF_YO = OFF_Z1 + 32 * 54 * 4 * 512;
constexpr u32 OFF_W3T = OFF_YO + 32 * 47 * 4 * 1024;
constexpr u32 OFF_KVP = OFF_W3T + 40 * 47 * 1024;
constexpr u32 OFF_KVF = OFF_KVP + 8 * 32 * 18 * 1024;
constexpr u32 WS_USED = OFF_KVF + 8 * 26 * 1024;
constexpr u32 L_UP = 81920;
constexpr u32 L_PARK = L_UP;
constexpr u32 L_STAT = L_UP + 16384;
constexpr u32 L_NRM = L_UP + 24576;
constexpr u32 L_PART = L_UP + 25600;
constexpr u32 L_SCR = L_UP + 65536;
constexpr u32 LDS_BYTES = 163840;
constexpr int SPIN = 1 << 15;

DI void glds(const void* sbase, u32 voff, u32 lds) {
  asm volatile("s_mov_b32 m0, %2\n\ts_nop 0\n\tglobal_load_lds_dwordx4 %0, %1" ::"v"(voff), "s"(sbase), "s"(lds) : "memory");
}
DI void glds_sc1(const void* sbase, u32 voff, u32 lds) {
  asm volatile("s_mov_b32 m0, %2\n\ts_nop 0\n\tglobal_load_lds_dwordx4 %0, %1 sc1" ::"v"(voff), "s"(sbase), "s"(lds) : "memory");
}
template <int OFF> DI v4u gld16(const void* sbase, u32 voff) {
  v4u r;
  asm volatile("global_load_dwordx4 %0, %1, %2 offset:%3" : "=v"(r) : "v"(voff), "s"(sbase), "i"(OFF) : "memory");
  return r;
}
template <int OFF> DI v4u gld16_sc1(const void* sbase, u32 voff) {
  v4u r;
  asm volatile("global_load_dwordx4 %0, %1, %2 offset:%3 sc1" : "=v"(r) : "v"(voff), "s"(sbase), "i"(OFF) : "memory");
  return r;
}
template <int OFF> DI v2u gld8_sc1(const void* sbase, u32 voff) {
  v2u r;
  asm volatile("global_load_dwordx2 %0, %1, %2 offset:%3 sc1" : "=v"(r) : "v"(voff), "s"(sbase), "i"(OFF) : "memory");
  return r;
}
DI u32 gld2(const void* sbase, u32 voff) {
  u32 r;
  asm volatile("s_nop 4\n\tglobal_load_ushort %0, %1, %2" : "=v"(r) : "v"(voff), "s"(sbase) : "memory");
  return r;
}
DI void st16(v4u v, __amdgpu_buffer_rsrc_t rs, u32 vo, u32 so) {
  __builtin_amdgcn_raw_buffer_store_b128(v, rs, vo, so, 16);
  asm volatile("s_nop 1" ::"v"(v));
}
template <int N> DI void wait_bar() { asm volatile("s_waitcnt vmcnt(%0) lgkmcnt(0)\n\ts_barrier" ::"i"(N) : "memory"); }
DI void bar() { asm volatile("s_waitcnt lgkmcnt(0)\n\ts_barrier" ::: "memory"); }
template <int N> DI void waitv() { asm volatile("s_waitcnt vmcnt(%0)" ::"i"(N) : "memory"); }
template <class T> DI void tie(T& a) { asm volatile("" : "+v"(a)); }
DI v4u lds16(u32 a) { return *(const LAS v4u*)(a); }
DI f4v ldsf4(u32 a) { return *(const LAS f4v*)(a); }
DI void sts16(u32 a, v4u v) { *(LAS v4u*)(a) = v; }
DI v2u trd(u32 a) { return __builtin_bit_cast(v2u, __builtin_amdgcn_ds_read_tr16_b64_v4i16((LAS s4v*)(a))); }
DI v4u trd2(u32 a0, u32 a1) { v2u x = trd(a0), y = trd(a1); return v4u{x.x, x.y, y.x, y.y}; }
DI u32 pk2(float a, float b) { f2v v = {a, b}; return __builtin_bit_cast(u32, __builtin_convertvector(v, b2v)); }
DI float asf(u32 p) { return __builtin_bit_cast(float, p); }
DI u32 asu(float p) { return __builtin_bit_cast(u32, p); }
DI float blo(u32 p) { return asf(p << 16); }
DI float bhi(u32 p) { return asf(p & 0xffff0000u); }
DI float dot2(u32 a, u32 b, float c) { return __builtin_amdgcn_fdot2_f32_bf16(__builtin_bit_cast(b2v, a), __builtin_bit_cast(b2v, b), c, false); }
DI f16v mfma32(v4u a, v4u b, f16v c) { return __builtin_amdgcn_mfma_f32_32x32x16_bf16(__builtin_bit_cast(s8v, a), __builtin_bit_cast(s8v, b), c, 0, 0, 0); }
DI f4v mfma16(v4u a, v4u b, f4v c) { return __builtin_amdgcn_mfma_f32_16x16x32_bf16(__builtin_bit_cast(s8v, a), __builtin_bit_cast(s8v, b), c, 0, 0, 0); }
DI f4v mfma16k(v2u a, v2u b, f4v c) { return __builtin_amdgcn_mfma_f32_16x16x16bf16_1k(__builtin_bit_cast(s4v, a), __builtin_bit_cast(s4v, b), c, 0, 0, 0); }
DI f16v zero16() { f16v z; for (int e = 0; e < 16; ++e) z[e] = 0.f; return z; }
DI float gelu(float v) {
  const float e = __builtin_amdgcn_exp2f(v * __builtin_fmaf(v * v, -0.10294324f, -2.3022082f));
  return v * __builtin_amdgcn_rcpf(1.0f + e);
}
DI float phi(float v) { return (v > 0.f ? v + 1.0f : __builtin_amdgcn_exp2f(v * 1.44269504f)) + 1e-6f; }
template <int CTRL> DI float dpp_add(float v) {
  return v + __builtin_bit_cast(float, __builtin_amdgcn_update_dpp(0, __builtin_bit_cast(int, v), CTRL, 0xf, 0xf, true));
}
DI float add32(float v) {
  auto r = __builtin_amdgcn_permlane32_swap(asu(v), asu(v), false, false);
  return asf(r[0]) + asf(r[1]);
}
DI float add16(float v) {
  auto r = __builtin_amdgcn_permlane16_swap(asu(v), asu(v), false, false);
  return asf(r[0]) + asf(r[1]);
}
DI int olane() {
  int l;
  asm volatile("v_mbcnt_lo_u32_b32 %0, -1, 0\n\tv_mbcnt_hi_u32_b32 %0, -1, %0" : "=v"(l));
  return l;
}
typedef const u8* cptr;
DI cptr karg(int i) {
  const __attribute__((address_space(4))) cptr* ka = (const __attribute__((address_space(4))) cptr*)__builtin_amdgcn_kernarg_segment_ptr();
  asm volatile("" : "+s"(ka));
  return ka[i];
}
DI int osc(int v) { asm volatile("" : "+s"(v)); return v; }
template <class F, int... I> DI void sfor_(F&& f, std::integer_sequence<int, I...>) { (f(std::integral_constant<int, I>{}), ...); }
template <int N, class F> DI void sfor(F&& f) { sfor_(f, std::make_integer_sequence<int, N>{}); }
DI bool tag_ok(v4u g, u32 t8) { return (((g.x ^ t8) | (g.y ^ t8) | (g.z ^ t8) | (g.w ^ t8)) & 0xffu) == 0; }
DI v4u tag_put(f4v c, u32 t8) { return v4u{(asu(c.x) & ~0xffu) | t8, (asu(c.y) & ~0xffu) | t8, (asu(c.z) & ~0xffu) | t8, (asu(c.w) & ~0xffu) | t8}; }

#define STAMP(j)
#define STAMPB(j)
#define CSTAMP(j)

constexpr int RB = 4;
constexpr int cmin(int a, int b) { return a < b ? a : b; }
constexpr int cnt_B(int NT, int j) {
  int n = NT * cmin(RB - 1, 15 - j);
  for (int m = (j - RB > 0 ? j - RB : 0); m < j; ++m) if (!(m & 1) && m / 2 + 4 < 8) n += 4;
  return cmin(n, 63);
}
constexpr int cnt_since_mid(int NT, int i0, int i1) {
  int n = 0;
  for (int j = 2 * i0 + 1; j <= 2 * i1; ++j) if (j + RB < 16) n += NT;
  for (int i = i0 + 1; i < i1; ++i) if (i + 4 < 8) n += 4;
  return cmin(n, 63);
}
constexpr u32 slotA(int i) { return (i & 3) == 0 ? 81920u : (i & 3) == 1 ? 114688u : (i & 3) == 2 ? 16384u : 49152u; }

struct Ctx {
  u8* ws;
  __amdgpu_buffer_rsrc_t wsr;
  u64 tagb;
  u32 t8;
  int w, rg, cb;
};

DI void post(const Ctx& c, int ev) {
  if (c.w == 4 && olane() == 0)
    __hip_atomic_store((u64*)(c.ws + OFF_FLAG + (osc(c.rg) * 8 + c.cb) * 8), c.tagb + ev, __ATOMIC_RELAXED, __HIP_MEMORY_SCOPE_AGENT);
}
DI bool flags_seen(const Ctx& c, v2u f, int ev) { const u64 d = (((u64)f.y << 32) | f.x) - c.tagb; return __all(d >= (u64)ev && d < 16); }
DI void spin_flags(const Ctx& c, u32 off, int ev) {
  _Pragma("clang loop unroll(disable)") for (int s = 0; s < SPIN; ++s) {
    if (flags_seen(c, __builtin_amdgcn_raw_buffer_load_b64(c.wsr, off, 0, 16), ev)) break;
    __builtin_amdgcn_s_sleep(1);
  }
}

DI void pack_tile(const f16v& acc, u32 (&p)[8]) {
#pragma unroll
  for (int e = 0; e < 8; ++e) p[e] = pk2(acc[2 * e], acc[2 * e + 1]);
}
DI void swap_tile(const u32 (&p)[8], v4u& o0, v4u& o1) {
  auto a = __builtin_amdgcn_permlane32_swap(p[0], p[2], false, false);
  auto c = __builtin_amdgcn_permlane32_swap(p[1], p[3], false, false);
  o0 = v4u{(u32)a[0], (u32)c[0], (u32)a[1], (u32)c[1]};
  auto d = __builtin_amdgcn_permlane32_swap(p[4], p[6], false, false);
  auto e = __builtin_amdgcn_permlane32_swap(p[5], p[7], false, false);
  o1 = v4u{(u32)d[0], (u32)e[0], (u32)d[1], (u32)e[1]};
}
DI u32 f_trb(int l) {
  const int q = (l & 15) >> 2, p = l & 3;
  return (8 * (l >> 4) + q) * 64 + (((p >> 1) ^ (q >> 1)) * 16) + (p & 1) * 8;
}
DI void z1_store(const Ctx& c, f16v& acc, int pw) {
#pragma unroll
  for (int e = 0; e < 16; ++e) acc[e] = gelu(acc[e]);
  u32 p[8]; v4u o0, o1;
  pack_tile(acc, p); swap_tile(p, o0, o1);
  const int l = olane(), t32 = l & 31;
  const u32 so = OFF_Z1 + ((c.rg * 54 + 2 * pw) * 4 + 2 * (c.w & 1)) * 512, vn = (t32 >> 4) * 512 + (l >> 5) * 256 + (t32 & 15) * 16;
  st16(o0, c.wsr, vn, so);
  st16(o1, c.wsr, vn + 2048, so);
}

template <int NT> DI void phase2(const Ctx& c, bool late, f16v& acc1, int pw_last, u64 epoch, v4u (&z1v)[2], u32& bsv) {
  u8* const ws = c.ws;
  const int w = c.w, rg = c.rg, cb = c.cb, q = w & 3, g = w >> 2;
  const int lane = olane();
  const int ftb = cb < 7 ? 7 * cb : 48, nta = cb < 6 ? 4 : 3;
  const u8* pz = ws + OFF_Z2T + (size_t)(2 * q * 54 + ftb + (g ? nta : 0)) * 1024;
  const u32 vl = lane * 16;
  v4u zb[RB][NT];
  auto loadZ = [&](int j) {
    const u8* p = pz + (size_t)((j >> 1) * 8 + (j & 1)) * 54 * 1024;
    zb[j % RB][0] = gld16_sc1<0>(p, vl); zb[j % RB][1] = gld16_sc1<1024>(p, vl); zb[j % RB][2] = gld16_sc1<2048>(p, vl);
    if (NT == 4) zb[j % RB][NT - 1] = gld16_sc1<3072>(p, vl);
  };
#pragma unroll
  for (int j = 0; j < RB; ++j) loadZ(j);
  SB();
  if (late) z1_store(c, acc1, pw_last);
  STAMP(6); STAMPB(16);
  wait_bar<0>();
  post(c, 4);
  if (blockIdx.x == 0 && w == 0 && lane == 0) __hip_atomic_store((u64*)ws, epoch, __ATOMIC_RELAXED, __HIP_MEMORY_SCOPE_AGENT);
  STAMP(7);
  const int t16 = lane & 15, g4 = lane >> 4;
  const u32 ra0 = q * 8192 + t16 * 128 + ((g4 ^ (t16 >> 1)) * 16), ra1 = q * 8192 + t16 * 128 + (((4 + g4) ^ (t16 >> 1)) * 16);
  const u8* pA = karg(4) + (size_t)(64 * rg) * 4096 + 16 * 128;
  const u32 voA = (8 * w + (lane >> 3)) * 4096 + (((lane & 7) ^ ((4 * w + (lane >> 4)) & 7)) * 16);
  auto fetchO = [&]() {
    const u8* p = ws + OFF_KVF + (size_t)(cb * 26 + q) * 1024;
#pragma unroll
    for (int k = 0; k < 7; ++k)
      if (k < 6 || q < 2) glds_sc1(p + k * 4096, vl, (k < 4 ? 0 : L_SCR - 16384) + (4 * k + q) * 1024);
  };
  f4v acc[NT][4];
  v4u af[2][4];
  v2u rgf = {0, 0}, pqf[5] = {{0, 0}, {0, 0}, {0, 0}, {0, 0}, {0, 0}};
#pragma unroll
  for (int tt = 0; tt < 4; ++tt) af[0][tt] = lds16(slotA(0) + ra0 + tt * 2048);
  sfor<16>([&](auto jc) {
    constexpr int j = jc.value, i = j >> 1;
    if (j & 1) {
      if (j < 15) {
#pragma unroll
        for (int tt = 0; tt < 4; ++tt) af[0][tt] = lds16(slotA(i + 1) + ra0 + tt * 2048);
      }
    } else {
#pragma unroll
      for (int tt = 0; tt < 4; ++tt) af[1][tt] = lds16(slotA(i) + ra1 + tt * 2048);
    }
    if (NT == 4) asm volatile("s_waitcnt vmcnt(%4)" : "+v"(zb[j % RB][0]), "+v"(zb[j % RB][1]), "+v"(zb[j % RB][2]), "+v"(zb[j % RB][NT - 1]) : "i"(cnt_B(NT, j)) : "memory");
    else asm volatile("s_waitcnt vmcnt(%3)" : "+v"(zb[j % RB][0]), "+v"(zb[j % RB][1]), "+v"(zb[j % RB][2]) : "i"(cnt_B(NT, j)) : "memory");
    SB();
#pragma unroll
    for (int n = 0; n < NT; ++n)
#pragma unroll
      for (int tt = 0; tt < 4; ++tt)
        acc[n][tt] = mfma16(zb[j % RB][n], af[j & 1][tt], j == 0 ? f4v{0.f, 0.f, 0.f, 0.f} : acc[n][tt]);
    SB();
    if (j + RB < 16) loadZ(j + RB);
    if (j == 13) {
      const int hw = 2 * w + (lane >> 5);
      const u8* p = ws + OFF_Z1 + (size_t)((rg * 54 + ftb) * 4) * 512;
      const u32 u1 = hw + 16 < 4 * (nta + 3) ? hw + 16 : hw;
      z1v[0] = gld16_sc1<0>(p, hw * 512 + (lane & 31) * 16);
      z1v[1] = gld16_sc1<0>(p, u1 * 512 + (lane & 31) * 16);
      bsv = gld2(karg(5), (64 * rg + 16 * (hw & 3) + t16) * 2);
    }
    if (!(j & 1) && i < 7) {
      if (i == 3) asm volatile("s_waitcnt vmcnt(%1) lgkmcnt(0)\n\ts_barrier" : "+v"(rgf) : "i"(cmin(cnt_since_mid(NT, 0, 3), cnt_since_mid(NT, 1, 3))) : "memory");
      else if (i == 4) wait_bar<cnt_since_mid(NT, 1, 4)>();
      else if (i == 5) wait_bar<cnt_since_mid(NT, 2, 5)>();
      else if (i == 6) wait_bar<cnt_since_mid(NT, 3, 6)>();
      else bar();
      if (i + 4 < 8) {
#pragma unroll
        for (int u = 0; u < 4; ++u) glds(pA + u * 128, voA, slotA(i) + u * 8192 + w * 1024);
        pA += 512;
      }
      if (i == 1) {
        rgf = gld8_sc1<0>(ws + OFF_FLAG + rg * 64, (lane & 7) * 8);
        if (NT == 3 && g == 1 && rg < 26) {
          const int src = rg < 13 ? rg : 13 + (rg - 13 < 5 ? rg - 13 : (rg - 13 < 9 ? rg - 18 : rg - 21));
          const u8* p = ws + OFF_KVP + (size_t)((cb * 18 + src) * 32 + 8 * q) * 1024;
#pragma unroll
          for (int k = 0; k < 8; ++k) glds_sc1(p + k * 1024, vl, (q < 2 ? 0 : L_SCR - 16384) + (8 * q + k) * 1024);
        }
      }
      if (i == 3) {
        if (!flags_seen(c, rgf, 4)) spin_flags(c, OFF_FLAG + rg * 64 + (lane & 7) * 8, 4);
        if (NT == 3 && g == 1 && rg < 26) {
          const int pr = rg < 13 ? rg : rg - 13;
          const int ti = pr < 5 ? pr : (pr < 9 ? pr - 5 : pr - 8), tj = pr < 5 ? pr : (pr < 9 ? pr - 4 : pr - 9);
          f4v s = {0.f, 0.f, 0.f, 0.f};
          bool ok = true;
#pragma unroll
          for (int k = 0; k < 8; ++k) {
            const v4u v = lds16((k < 4 ? 0 : L_SCR - 16384) + (g4 + 4 * k) * 1024 + (16 * q + t16) * 16);
            ok = ok && tag_ok(v, c.t8);
            s += f4v{asf(v.x), asf(v.y), asf(v.z), asf(v.w)};
          }
          if (!__all(ok)) {
            const int src = rg < 13 ? rg : 13 + ti;
            _Pragma("clang loop unroll(disable)") for (int sp = 0; sp < SPIN; ++sp) {
              s = f4v{0.f, 0.f, 0.f, 0.f}; ok = true;
#pragma unroll
              for (int k = 0; k < 8; ++k) {
                const v4u v = __builtin_amdgcn_raw_buffer_load_b128(c.wsr, (g4 + 4 * k) * 1024 + (16 * q + t16) * 16, OFF_KVP + (cb * 18 + src) * 32 * 1024, 16);
                ok = ok && tag_ok(v, c.t8);
                s += f4v{asf(v.x), asf(v.y), asf(v.z), asf(v.w)};
              }
              if (__all(ok)) break;
              __builtin_amdgcn_s_sleep(1);
            }
          }
          const int he = (16 * tj + t16) / 20;
#pragma unroll
          for (int e = 0; e < 4; ++e) {
            const float v = add32(add16(s[e]));
            s[e] = (16 * ti + 4 * q + e) / 20 == he ? v : 0.f;
          }
          if (lane < 16) st16(tag_put(s, c.t8), c.wsr, (16 * q + lane) * 16, OFF_KVF + (cb * 26 + rg) * 1024);
        }
      }
      if (i == 5 && NT == 3 && g == 1) {
        fetchO();
        const u8* pp = ws + OFF_PQ + (size_t)((rg * 40 + 5 * cb) * 4 + q) * 512;
        const u32 vo = (g4 >> 1) * 256 + t16 * 16 + (g4 & 1) * 8;
        pqf[0] = gld8_sc1<0>(pp, vo); pqf[1] = gld8_sc1<2048>(pp, vo);
        pqf[2] = gld8_sc1<0>(pp + 4096, vo); pqf[3] = gld8_sc1<2048>(pp + 4096, vo); pqf[4] = gld8_sc1<0>(pp + 8192, vo);
      }
    }
    if (j == 14) {
      if (NT == 3) {
        asm volatile("s_waitcnt vmcnt(3)" : "+v"(pqf[0]), "+v"(pqf[1]), "+v"(pqf[2]), "+v"(pqf[3]), "+v"(pqf[4]) :: "memory");
        if (g == 1) {
          _Pragma("clang loop unroll(disable)") for (int sp = 0; sp < SPIN; ++sp) {
            bool ok = true;
#pragma unroll
            for (int k = 0; k < 7; ++k) {
              const int x = k < 6 || q < 2 ? 4 * k + q : q;
              ok = ok && tag_ok(lds16((x < 16 ? 0 : L_SCR - 16384) + x * 1024 + vl), c.t8);
            }
            if (__all(ok)) break;
            __builtin_amdgcn_s_sleep(1);
            fetchO();
            waitv<0>();
          }
        }
      }
      bar();
      if (NT == 3 && g == 1) {
        auto tile = [&](int x) -> v2u {
          const f4v v = ldsf4((x < 16 ? 0 : L_SCR - 16384) + x * 1024 + vl);
          return v2u{pk2(v.x, v.y), pk2(v.z, v.w)};
        };
#pragma unroll
        for (int tj = 0; tj < 5; ++tj) {
          f4v num = mfma16k(tile(tj), pqf[tj], f4v{0.f, 0.f, 0.f, 0.f}), den = mfma16k(tile(13 + tj), pqf[tj], f4v{0.f, 0.f, 0.f, 0.f});
          if (tj > 0) { num = mfma16k(tile(5 + tj - 1), pqf[tj - 1], num); den = mfma16k(tile(18 + tj - 1), pqf[tj - 1], den); }
          if (tj < 4) { num = mfma16k(tile(9 + tj), pqf[tj + 1], num); den = mfma16k(tile(22 + tj), pqf[tj + 1], den); }
          const v2u o = {pk2(num.x * __builtin_amdgcn_rcpf(den.x + 1e-6f), num.y * __builtin_amdgcn_rcpf(den.y + 1e-6f)),
                         pk2(num.z * __builtin_amdgcn_rcpf(den.z + 1e-6f), num.w * __builtin_amdgcn_rcpf(den.w + 1e-6f))};
          const int cc = 80 * cb + 16 * tj + 4 * g4;
          __builtin_amdgcn_raw_buffer_store_b64(o, c.wsr, (cc >> 5) * 4096 + (((cc & 31) >> 3) * 16 + t16) * 16 + ((cc >> 2) & 1) * 8,
                                                OFF_YO + (rg * 47 * 4 + q) * 1024, 16);
        }
      }
    }
  });
  STAMP(8); STAMPB(17);
  asm volatile("s_waitcnt vmcnt(0) lgkmcnt(0)\n\ts_barrier" : "+v"(z1v[0]), "+v"(z1v[1]), "+v"(bsv) :: "memory");
  post(c, 5);
  {
    const u32 rb = (q * 28 + (g ? nta : 0) * 4) * 1024 + vl;
#pragma unroll
    for (int n = 0; n < NT; ++n)
#pragma unroll
      for (int tt = 0; tt < 4; ++tt) *(LAS f4v*)(rb + (n * 4 + tt) * 1024) = acc[n][tt];
  }
}

DI void phase3(const Ctx& c, v4u (&z1v)[2], u32 bsv) {
  u8* const ws = c.ws;
  const int w = c.w, rg = c.rg, cb = c.cb;
  const int lane = olane(), t16 = lane & 15;
  const int ftb = cb < 7 ? 7 * cb : 48, nta = cb < 6 ? 4 : 3;
  const u32 vl = lane * 16;
  const u8* pw3 = ws + OFF_W3T + (size_t)(cb * 47) * 5120 + 2048;
  v4u wq[6][5];
  auto loadW = [&](int s, int ks) {
    const u8* p = pw3 + (size_t)ks * 5120;
    wq[s][0] = gld16_sc1<-2048>(p, vl); wq[s][1] = gld16_sc1<-1024>(p, vl); wq[s][2] = gld16_sc1<0>(p, vl);
    wq[s][3] = gld16_sc1<1024>(p, vl); wq[s][4] = gld16_sc1<2048>(p, vl);
  };
  loadW(0, 2 * w); loadW(1, 2 * w + 1);
  SB();
  bar();
  {
    const int hw = 2 * w + (lane >> 5), gg = (lane >> 4) & 1;
    const float bb = blo(bsv);
#pragma unroll
    for (int k = 0; k < 2; ++k) {
      const int u = hw + 16 * k;
      if (k == 0 || u < 4 * (nta + 3)) {
        const u32 a = u * 1024 + (t16 + 32 * gg) * 16;
        const f4v lo = (ldsf4(a) + ldsf4(a + 28672)) + (ldsf4(a + 2 * 28672) + ldsf4(a + 3 * 28672));
        const f4v hi = (ldsf4(a + 256) + ldsf4(a + 28672 + 256)) + (ldsf4(a + 2 * 28672 + 256) + ldsf4(a + 3 * 28672 + 256));
        const v4u z = z1v[k];
        const v4u y = {pk2(blo(z.x) * (lo.x + bb), bhi(z.x) * (lo.y + bb)), pk2(blo(z.y) * (lo.z + bb), bhi(z.y) * (lo.w + bb)),
                       pk2(blo(z.z) * (hi.x + bb), bhi(z.z) * (hi.y + bb)), pk2(blo(z.w) * (hi.z + bb), bhi(z.w) * (hi.w + bb))};
        const int ft = ftb + (u >> 2);
        st16(y, c.wsr, (20 + (ft >> 1)) * 4096 + (u & 3) * 1024 + ((2 * (ft & 1) + gg) * 16 + t16) * 16,
                                               OFF_YO + rg * 47 * 4096);
      }
    }
  }
  SB();
  v2u f1 = gld8_sc1<0>(ws + OFF_FLAG + rg * 64, (lane & 7) * 8);
  const int ks2 = 16 + 4 * w;
  loadW(2, ks2); loadW(3, ks2 + 1); loadW(4, ks2 + 2); loadW(5, w < 7 ? ks2 + 3 : ks2);
  asm volatile("s_waitcnt vmcnt(20) lgkmcnt(0)\n\ts_barrier" : "+v"(f1) :: "memory");
  post(c, 6);
  STAMP(9);
  if (!flags_seen(c, f1, 5)) spin_flags(c, OFF_FLAG + rg * 64 + (lane & 7) * 8, 5);
  const u8* py = ws + OFF_YO + (size_t)(rg * 47) * 4096;
  v4u ya[6][4];
  auto loadY = [&](int s, int ks) {
    const u8* p = py + (size_t)ks * 4096;
    ya[s][0] = gld16_sc1<0>(p, vl); ya[s][1] = gld16_sc1<1024>(p, vl); ya[s][2] = gld16_sc1<2048>(p, vl); ya[s][3] = gld16_sc1<3072>(p, vl);
  };
  loadY(0, 2 * w); loadY(1, 2 * w + 1);
  f1 = gld8_sc1<0>(ws + OFF_FLAG + rg * 64, (lane & 7) * 8);
  f4v oc[5][4];
  auto step = [&](int s) {
#pragma unroll
    for (int n = 0; n < 5; ++n)
#pragma unroll
      for (int tt = 0; tt < 4; ++tt) oc[n][tt] = mfma16(wq[s][n], ya[s][tt], s == 0 ? f4v{0.f, 0.f, 0.f, 0.f} : oc[n][tt]);
  };
#define TIE_STEP(s, N) asm volatile("s_waitcnt vmcnt(%9)" : "+v"(wq[s][0]), "+v"(wq[s][1]), "+v"(wq[s][2]), "+v"(wq[s][3]), "+v"(wq[s][4]), \
                                    "+v"(ya[s][0]), "+v"(ya[s][1]), "+v"(ya[s][2]), "+v"(ya[s][3]) : "i"(N) : "memory")
  TIE_STEP(0, 5); SB(); step(0); SB();
  TIE_STEP(1, 1); SB(); step(1); SB();
  asm volatile("s_waitcnt vmcnt(0)" : "+v"(f1) :: "memory");
  if (!flags_seen(c, f1, 6)) spin_flags(c, OFF_FLAG + rg * 64 + (lane & 7) * 8, 6);
  STAMP(10);
  loadY(2, ks2); loadY(3, ks2 + 1); loadY(4, ks2 + 2); loadY(5, w < 7 ? ks2 + 3 : ks2);
  const int tid = w * 64 + lane;
  const int t0 = tid / 10, c0 = tid - t0 * 10, u1 = tid < 128 ? tid + 512 : tid, t1 = u1 / 10, c1 = u1 - t1 * 10;
  const u8* px = karg(0) + (size_t)(64 * rg) * 1280 + cb * 160;
  v4u xv0 = gld16<0>(px, t0 * 1280 + c0 * 16), xv1 = gld16<0>(px, t1 * 1280 + c1 * 16);
  TIE_STEP(2, 14); SB(); step(2); SB();
  TIE_STEP(3, 10); SB(); step(3); SB();
  TIE_STEP(4, 6); SB(); step(4); SB();
  TIE_STEP(5, 2); SB();
  if (w < 7) step(5);
  SB();
  STAMP(11);
#pragma unroll
  for (int n = 0; n < 5; ++n)
#pragma unroll
    for (int tt = 0; tt < 4; ++tt) *(LAS f4v*)((w * 20 + n * 4 + tt) * 1024 + vl) = oc[n][tt];
  asm volatile("s_waitcnt vmcnt(0) lgkmcnt(0)\n\ts_barrier" : "+v"(xv0), "+v"(xv1) :: "memory");
  u8* const po = (u8*)karg(7);
  auto finish = [&](int t, int ch, v4u xv) {
    const u32 a = ((ch >> 1) * 4 + (t >> 4)) * 1024 + ((t & 15) + 32 * (ch & 1)) * 16;
    f4v lo = ldsf4(a), hi = ldsf4(a + 256);
#pragma unroll
    for (int k = 1; k < 8; ++k) { lo += ldsf4(a + k * 20480); hi += ldsf4(a + k * 20480 + 256); }
    const v4u o = {pk2(blo(xv.x) + lo.x, bhi(xv.x) + lo.y), pk2(blo(xv.y) + lo.z, bhi(xv.y) + lo.w),
                   pk2(blo(xv.z) + hi.x, bhi(xv.z) + hi.y), pk2(blo(xv.w) + hi.z, bhi(xv.w) + hi.w)};
    *(__attribute__((address_space(1))) v4u*)(po + (size_t)(64 * rg + t) * 1280 + cb * 160 + ch * 16) = o;
  };
  finish(t0, c0, xv0);
  if (w < 2) finish(t1, c1, xv1);
  STAMP(12); CSTAMP(31);
}

__global__ __launch_bounds__(512, 2) void fused(const u8* __restrict__ x, const u8* __restrict__ wqkv, const u8* __restrict__ wo,
                                                const u8* __restrict__ win, const u8* __restrict__ wsp, const u8* __restrict__ bsp,
                                                const u8* __restrict__ wout, u8* __restrict__ out, u8* ws) {
  extern __shared__ __attribute__((aligned(16))) u8 smem[];
  const int lane = threadIdx.x & 63;
  const int w = __builtin_amdgcn_readfirstlane(threadIdx.x >> 6);
  const int b = blockIdx.x, xg = b & 7, jb = b >> 3;
  const int rg = 8 * (xg >> 1) + (jb & 7), cb = 4 * (xg & 1) + (jb >> 3);
  const int g = w >> 2, wq4 = w & 3, mh = w & 1, hf = (w >> 1) & 1;
  Ctx c;
  c.ws = ws; c.w = w; c.rg = rg; c.cb = cb;
  c.wsr = __builtin_amdgcn_make_buffer_rsrc(ws, 0, WS_USED, 0x00020000);
  const auto wsr = c.wsr;
  STAMP(0); CSTAMP(30);
  const int r8 = lane >> 3, s8 = lane & 7;
  v4u xr[10];
  {
    const u8* xb = x + (size_t)(64 * rg + 8 * w) * 1280;
    const u32 vo = r8 * 1280 + s8 * 16;
    xr[0] = gld16<0>(xb, vo); xr[1] = gld16<128>(xb, vo); xr[2] = gld16<256>(xb, vo); xr[3] = gld16<384>(xb, vo); xr[4] = gld16<512>(xb, vo);
    xr[5] = gld16<640>(xb, vo); xr[6] = gld16<768>(xb, vo); xr[7] = gld16<896>(xb, vo); xr[8] = gld16<1024>(xb, vo); xr[9] = gld16<1152>(xb, vo);
  }
  v2u ep = gld8_sc1<0>(ws, 0u);
  const u32 blks = g == 0 ? (u32)(43 + cb) | (u32)(12 + cb) << 8 | (u32)(cb < 2 ? 28 + cb : cb - 2) << 16 | (u32)(34 + cb) << 24
                          : (u32)(cb < 6 ? 51 + cb : 4 + cb) | (u32)(20 + cb) << 8 | (u32)(cb < 4 ? 6 + cb : 26 + cb) << 16 | 42u << 24;
  const int nr = (g == 0 || cb == 7) ? 4 : 3;
  const bool late = (g == 0) != (cb == 7);
  const u8* pw;
  u32 stw, vo0, vo1;
  auto set_desc = [&](int r) {
    const int l = olane(), rr = l >> 3, ss = l & 7;
    if (r < nr) {
      const int bk = (blks >> (8 * r)) & 0xff;
      const bool isq = bk < 30;
      pw = (isq ? wqkv : win - 30 * 128) + bk * 128;
      const u32 d = isq ? 3840 : 3456;
      stw = 64 * d;
      vo0 = (16 * wq4 + rr) * d + ((ss ^ (((rr >> 1) & 1) << 2)) * 16);
      vo1 = vo0 + 8 * d;
    } else {
      pw = wsp + (size_t)(64 * rg) * 4096 + (g == 0 ? 11 : 6) * 128;
      stw = 128;
      vo0 = (16 * wq4 + rr) * 4096 + ((ss ^ (rr >> 1)) * 16);
      vo1 = (vo0 ^ 64) + 8 * 4096;
    }
  };
  set_desc(0);
  const u32 ring = g == 0 ? 40960 : 0;
  const u32 wl = ring + wq4 * 2048;
#pragma unroll
  for (int i = 0; i < 5; ++i) { glds(pw, vo0, wl + i * 8192); glds(pw, vo1, wl + i * 8192 + 1024); pw += stw; }
  asm volatile("s_waitcnt vmcnt(10)"
               : "+v"(xr[0]), "+v"(xr[1]), "+v"(xr[2]), "+v"(xr[3]), "+v"(xr[4]), "+v"(xr[5]), "+v"(xr[6]), "+v"(xr[7]), "+v"(xr[8]), "+v"(xr[9]), "+v"(ep)
               :: "memory");
  STAMP(1);
  const u64 epoch = (((u64)(u32)__builtin_amdgcn_readfirstlane(ep.y) << 32) | (u32)__builtin_amdgcn_readfirstlane(ep.x)) + 1;
  c.tagb = epoch << 4;
  c.t8 = (u32)epoch & 0xffu;
  const u32 tag32 = (u32)epoch * 0x9E3779B1u ^ (u32)(epoch >> 32);
  {
    float a1[4] = {0.f, 0.f, 0.f, 0.f}, a2[4] = {0.f, 0.f, 0.f, 0.f};
#pragma unroll
    for (int j = 0; j < 10; ++j)
#pragma unroll
      for (int e = 0; e < 4; ++e) { const u32 v = xr[j][e]; a1[e] = dot2(v, 0x3f803f80u, a1[e]); a2[e] = dot2(v, v, a2[e]); }
    float s1 = (a1[0] + a1[1]) + (a1[2] + a1[3]), s2 = (a2[0] + a2[1]) + (a2[2] + a2[3]);
    s1 = dpp_add<0xB1>(s1); s2 = dpp_add<0xB1>(s2); s1 = dpp_add<0x4E>(s1); s2 = dpp_add<0x4E>(s2); s1 = dpp_add<0x141>(s1); s2 = dpp_add<0x141>(s2);
    const float mean = s1 * (1.0f / 640.0f);
    const float rs = __builtin_amdgcn_rsqf(fmaxf(s2 * (1.0f / 640.0f) - mean * mean, 0.f) + 1e-5f), sh = -mean * rs;
    const int row = 8 * w + r8;
    const u32 rb = L_UP + row * 1280;
#pragma unroll
    for (int j = 0; j < 10; ++j) {
      v4u o;
#pragma unroll
      for (int e = 0; e < 4; ++e) o[e] = pk2(__builtin_fmaf(blo(xr[j][e]), rs, sh), __builtin_fmaf(bhi(xr[j][e]), rs, sh));
      sts16(rb + (j >> 1) * 256 + ((((s8 + 8 * j) ^ row) & 15) * 16), o);
    }
  }
  bar();
  STAMP(2);
  v4u xa[40];
  {
    const int row = 32 * mh + (lane & 31);
    const u32 rb = L_UP + row * 1280, X = (u32)((lane >> 5) ^ (row & 15));
#pragma unroll
    for (int ks = 0; ks < 40; ++ks) xa[ks] = lds16(rb + (ks >> 3) * 256 + ((((2 * ks) & 15) ^ X) * 16));
  }
  const u32 tb = ring + (8 * (lane >> 5) + ((lane & 15) >> 2)) * 128 + ((hf ^ ((lane & 15) >> 3)) * 64) + ((lane >> 4) & 1) * 32 + (lane & 3) * 8;
  v4u wf[2][4];
  wait_bar<8>();
  if (g == 1) {
    const bool job = b < 235;
    const int ks = b / 5, ff = b - ks * 5;
    if (job) {
      const u8* src = (ks < 20 ? wo + (size_t)ks * 32 * 1280 : wout + (size_t)(ks - 20) * 32 * 1280) + ff * 256;
      const u32 vo = (8 * wq4 + (lane >> 4)) * 1280 + (lane & 15) * 16;
      glds(src, vo, L_SCR + wq4 * 2048);
      glds(src, vo + 4 * 1280, L_SCR + wq4 * 2048 + 1024);
    }
    bar(); bar(); bar();
    wait_bar<0>();
    if (job) {
#pragma unroll
      for (int k = 0; k < 2; ++k) {
        const int ct = ff * 8 + 2 * wq4 + k, cq = ct / 5;
        const u32 a = L_SCR + (8 * (lane >> 4) + ((lane & 15) >> 2)) * 256 + (2 * wq4 + k) * 32 + (lane & 3) * 8;
        st16(trd2(a, a + 1024), wsr, lane * 16, OFF_W3T + ((cq * 47 + ks) * 5 + ct - 5 * cq) * 1024);
      }
    }
  }
#pragma unroll
  for (int kk = 0; kk < 4; ++kk) wf[0][kk] = trd2(tb + kk * 2048, tb + kk * 2048 + 512);
  STAMP(3);

  f16v acc;
  for (int r = 0;; ++r) {
#pragma unroll
    for (int i = 0; i < 10; ++i) {
      wait_bar<6>();
      const u32 ra = tb + ((i + 1) % 5) * 8192;
      SB();
      acc = mfma32(wf[i & 1][0], xa[4 * i], i == 0 ? zero16() : acc);
      SB();
      wf[(i + 1) & 1][0] = trd2(ra, ra + 512);
      wf[(i + 1) & 1][1] = trd2(ra + 2048, ra + 2048 + 512);
      if (i == 5) set_desc(r + 1);
      glds(pw, vo0, wl + (i % 5) * 8192);
      SB();
      acc = mfma32(wf[i & 1][1], xa[4 * i + 1], acc);
      SB();
      wf[(i + 1) & 1][2] = trd2(ra + 4096, ra + 4096 + 512);
      wf[(i + 1) & 1][3] = trd2(ra + 6144, ra + 6144 + 512);
      glds(pw, vo1, wl + (i % 5) * 8192 + 1024); pw += stw;
      SB();
      acc = mfma32(wf[i & 1][2], xa[4 * i + 2], acc);
      SB();
      if (i == 6 && r == 1 && g == 1) {
        const u8* p = ws + OFF_STATS + (osc(rg) * 8 + 2 * wq4) * 1024;
        const u32 vo = olane() * 16;
        glds_sc1(p, vo, L_STAT + wq4 * 2048);
        glds_sc1(p + 1024, vo, L_STAT + wq4 * 2048 + 1024);
      }
      if (i == 3 && r == 2) post(c, 2);
      SB();
      acc = mfma32(wf[i & 1][3], xa[4 * i + 3], acc);
      SB();
    }
    if (late && r == 3) break;
    STAMP(20 + r);
    const int pc = ((blks >> (8 * r)) & 0xff) * 2 + hf;
    wait_bar<6>();
    if (r == 1 && w == 4) {
      const int l = olane();
      float s1 = 0.f, s2 = 0.f;
      bool ok = true;
#pragma unroll
      for (int e = 0; e < 8; ++e) {
        const v4u v = lds16(L_STAT + e * 1024 + l * 16);
        ok = ok && v.y == tag32 && v.w == tag32;
        s1 += asf(v.x); s2 += asf(v.z);
      }
      if (!__all(ok)) {
        _Pragma("clang loop unroll(disable)") for (int sp = 0; sp < SPIN; ++sp) {
          s1 = 0.f; s2 = 0.f; ok = true;
#pragma unroll
          for (int e = 0; e < 8; ++e) {
            const v4u v = __builtin_amdgcn_raw_buffer_load_b128(wsr, l * 16 + e * 1024, OFF_STATS + osc(rg) * 8192, 16);
            ok = ok && v.y == tag32 && v.w == tag32;
            s1 += asf(v.x); s2 += asf(v.z);
          }
          if (__all(ok)) break;
          __builtin_amdgcn_s_sleep(1);
        }
      }
      const float mean = s1 * (1.0f / 864.0f);
      const float rs = __builtin_amdgcn_rsqf(fmaxf(s2 * (1.0f / 864.0f) - mean * mean, 0.f) + 1e-5f);
      *(LAS float*)(L_NRM + l * 4) = rs;
      *(LAS float*)(L_NRM + 256 + l * 4) = -mean * rs;
    }
    if (pc >= 60) {
#pragma unroll
      for (int e = 0; e < 6; ++e) acc[e] = gelu(acc[e]);
      SB(); bar(); SB();
#pragma unroll
      for (int e = 6; e < 11; ++e) acc[e] = gelu(acc[e]);
      SB(); bar(); SB();
#pragma unroll
      for (int e = 11; e < 16; ++e) acc[e] = gelu(acc[e]);
    } else if (pc < 40) {
#pragma unroll
      for (int e = 0; e < 6; ++e) acc[e] = phi(acc[e]);
      SB(); bar(); SB();
#pragma unroll
      for (int e = 6; e < 11; ++e) acc[e] = phi(acc[e]);
      SB(); bar(); SB();
#pragma unroll
      for (int e = 11; e < 16; ++e) acc[e] = phi(acc[e]);
    } else {
      bar(); bar();
    }
    {
      const int l = olane(), t32 = l & 31, hh = l >> 5;
      u32 pk[8];
      pack_tile(acc, pk);
      const bool isz2 = pc >= 87;
      if (isz2) {
        float s1 = 0.f, s2 = 0.f;
#pragma unroll
        for (int e = 0; e < 8; ++e) { s1 = dot2(pk[e], 0x3f803f80u, s1); s2 = dot2(pk[e], pk[e], s2); }
        s1 = add32(s1); s2 = add32(s2);
        if (hh == 0) *(LAS f2v*)(L_PART + ((w >> 1) * 64 + 32 * mh + t32) * 8) = f2v{s1, s2};
      }
      SB(); bar(); SB();
      if (r == 2) post(c, 3);
      v4u o0, o1;
      swap_tile(pk, o0, o1);
      if (pc < 20 || (pc >= 60 && !isz2)) {
        const u32 so = pc < 20 ? OFF_PQ + ((osc(rg) * 40 + 2 * pc) * 4 + 2 * mh) * 512 : OFF_Z1 + ((osc(rg) * 54 + 2 * (pc - 60)) * 4 + 2 * mh) * 512;
        const u32 vn = (t32 >> 4) * 512 + hh * 256 + (t32 & 15) * 16;
        st16(o0, wsr, vn, so);
        st16(o1, wsr, vn + 2048, so);
      } else {
        const u32 ib = (isz2 ? L_PARK : L_SCR) + w * 2048 + t32 * 64, imx = (u32)((t32 >> 1) & 3);
        sts16(ib + ((hh ^ imx) * 16), o0);
        sts16(ib + (((2 + hh) ^ imx) * 16), o1);
        if (!isz2) {
          const u32 trb = L_SCR + w * 2048 + f_trb(l);
          const v4u f0 = trd2(trb, trb + 256 + 32), f1 = trd2(trb + 32, trb + 256);
          const u32 so = OFF_PKV + ((osc(rg) * 80 + 2 * (pc - 20)) * 2 + mh) * 1024;
          st16(f0, wsr, l * 16, so);
          st16(f1, wsr, l * 16 + 2048, so);
        }
      }
      if (r == 0 && w == 4) {
        float s1 = 0.f, s2 = 0.f;
#pragma unroll
        for (int e = 0; e < 4; ++e) {
          const f2v t = *(const LAS f2v*)(L_PART + (e * 64 + l) * 8);
          const bool use = e == 0 ? cb > 0 : (e == 1 ? true : cb < 6);
          s1 += use ? t.x : 0.f; s2 += use ? t.y : 0.f;
        }
        st16(v4u{asu(s1), tag32, asu(s2), tag32}, wsr, l * 16, OFF_STATS + (osc(rg) * 8 + cb) * 1024);
      }
      if (r == 1 && g == 1) {
#pragma unroll 1
        for (int k = 0; k < 2; ++k) {
          bar();
          const int pz = 2 * (k == 0 ? cb : cb + 8) + hf - 1;
          if (pz >= 0 && pz < 27) {
            const u32 trb = L_PARK + (4 * k + wq4) * 2048 + f_trb(l);
            const v4u f0 = trd2(trb, trb + 256 + 32), f1 = trd2(trb + 32, trb + 256);
            const u32 na = L_NRM + (32 * mh + 8 * (l >> 4)) * 4;
            const f4v c0 = ldsf4(na), c1 = ldsf4(na + 16), h0 = ldsf4(na + 256), h1 = ldsf4(na + 272);
            auto nrm = [&](v4u f) -> v4u {
              return v4u{pk2(__builtin_fmaf(blo(f.x), c0.x, h0.x), __builtin_fmaf(bhi(f.x), c0.y, h0.y)),
                         pk2(__builtin_fmaf(blo(f.y), c0.z, h0.z), __builtin_fmaf(bhi(f.y), c0.w, h0.w)),
                         pk2(__builtin_fmaf(blo(f.z), c1.x, h1.x), __builtin_fmaf(bhi(f.z), c1.y, h1.y)),
                         pk2(__builtin_fmaf(blo(f.w), c1.z, h1.z), __builtin_fmaf(bhi(f.w), c1.w, h1.w))};
            };
            const u32 so = OFF_Z2T + ((2 * osc(rg) + mh) * 54 + 2 * pz) * 1024;
            st16(nrm(f0), wsr, l * 16, so);
            st16(nrm(f1), wsr, l * 16 + 1024, so);
          }
        }
      }
    }
    if (r + 1 == nr) break;
  }
  STAMP(4); STAMPB(14);
  {
    const int l = olane();
    const u32 f2off = ((4 * (l >> 3) + wq4) * 8 + (l & 7)) * 8;
    bool seen;
    if (late) {
      v2u f2 = gld8_sc1<0>(ws + OFF_FLAG, f2off);
      asm volatile("s_waitcnt vmcnt(0)" : "+v"(f2) :: "memory");
      seen = flags_seen(c, f2, 2);
    } else {
      v2u f2 = gld8_sc1<0>(ws + OFF_FLAG, f2off);
      v2u f3 = gld8_sc1<0>(ws + OFF_FLAG + rg * 64, (l & 7) * 8);
      const u8* pa = wsp + (size_t)(64 * rg) * 4096;
      const u32 va0 = (16 * wq4 + (l >> 3)) * 4096 + (((l & 7) ^ (l >> 4)) * 16), va1 = (va0 ^ 64) + 8 * 4096;
#pragma unroll
      for (int u = 0; u < 4; ++u) { glds(pa + u * 128, va0, L_UP + u * 8192 + wq4 * 2048); glds(pa + u * 128, va1, L_UP + u * 8192 + wq4 * 2048 + 1024); }
      bar();
      asm volatile("s_waitcnt vmcnt(8)" : "+v"(f2), "+v"(f3) :: "memory");
      seen = flags_seen(c, f2, 2);
      bar();
      if (cb < 7) { bar(); bar(); }
      if (!flags_seen(c, f3, 3)) spin_flags(c, OFF_FLAG + rg * 64 + (l & 7) * 8, 3);
      const u8* pk = ws + OFF_PKV + (size_t)(rg * 80 + 5 * cb + wq4) * 2048;
      const u32 vl = l * 16;
      v4u k0 = gld16_sc1<0>(pk, vl), k1 = gld16_sc1<1024>(pk, vl), k2 = gld16_sc1<2048>(pk, vl), k3 = gld16_sc1<3072>(pk, vl);
      v4u v0 = gld16_sc1<0>(pk + 81920, vl), v1 = gld16_sc1<1024>(pk + 81920, vl), v2 = gld16_sc1<2048>(pk + 81920, vl), v3 = gld16_sc1<3072>(pk + 81920, vl);
#pragma unroll
      for (int u = 4; u < 8; ++u) { glds(pa + u * 128, va0, L_UP + u * 8192 + wq4 * 2048); glds(pa + u * 128, va1, L_UP + u * 8192 + wq4 * 2048 + 1024); }
      asm volatile("s_waitcnt vmcnt(8)" : "+v"(k0), "+v"(k1), "+v"(k2), "+v"(k3), "+v"(v0), "+v"(v1), "+v"(v2), "+v"(v3) :: "memory");
      SB();
      const f4v z4 = {0.f, 0.f, 0.f, 0.f};
      const v4u one = {0x3f803f80u, 0x3f803f80u, 0x3f803f80u, 0x3f803f80u};
      const u32 so = OFF_KVP + (cb * 18 * 32 + rg) * 1024;
      auto put = [&](f4v t, int x) { st16(tag_put(t, c.t8), wsr, vl, so + x * 32768); };
      put(mfma16(k1, v3, mfma16(k0, v2, z4)), 5 + wq4);
      put(mfma16(k3, v1, mfma16(k2, v0, z4)), 9 + wq4);
      put(mfma16(k3, v3, mfma16(k2, v2, z4)), wq4 + 1);
      put(mfma16(k3, one, mfma16(k2, one, z4)), 14 + wq4);
      if (wq4 == 0) {
        put(mfma16(k1, v1, mfma16(k0, v0, z4)), 0);
        put(mfma16(k1, one, mfma16(k0, one, z4)), 13);
      }
    }
    if (!seen) spin_flags(c, OFF_FLAG + f2off, 2);
  }
  STAMP(5); STAMPB(15);
  const int pw_last = (int)(blks >> 24) * 2 + hf - 60;
  v4u z1v[2];
  u32 bsv;
  if (g == 0 && cb < 6) phase2<4>(c, late, acc, pw_last, epoch, z1v, bsv);
  else phase2<3>(c, late, acc, pw_last, epoch, z1v, bsv);
  phase3(c, z1v, bsv);
}

extern "C" void kernel_launch(void* const* d_in, const int* in_sizes, int n_in, void* d_out, int out_size, void* d_ws, size_t ws_size,
                              hipStream_t stream) {
  (void)in_sizes; (void)n_in; (void)out_size; (void)ws_size;
  (void)hipFuncSetAttribute((const void*)fused, hipFuncAttributeMaxDynamicSharedMemorySize, LDS_BYTES);
  fused<<<256, 512, LDS_BYTES, stream>>>((const u8*)d_in[0], (const u8*)d_in[1], (const u8*)d_in[2], (const u8*)d_in[4], (const u8*)d_in[5],
                                         (const u8*)d_in[6], (const u8*)d_in[7], (u8*)d_out, (u8*)d_ws);
}
```

```cpp
#include <hip/hip_runtime.h>
#include <hip/hip_bf16.h>
#include <utility>
#define NOT2

typedef unsigned int u32;
typedef unsigned long long u64;
typedef unsigned char u8;
typedef u32 v4u __attribute__((ext_vector_type(4)));
typedef u32 v2u __attribute__((ext_vector_type(2)));
typedef float f16v __attribute__((ext_vector_type(16)));
typedef float f4v __attribute__((ext_vector_type(4)));
typedef float f2v __attribute__((ext_vector_type(2)));
typedef short s8v __attribute__((ext_vector_type(8)));
typedef short s4v __attribute__((ext_vector_type(4)));
typedef __bf16 b2v __attribute__((ext_vector_type(2)));
#pragma clang diagnostic ignored "-Wint-to-pointer-cast"
#define LAS __attribute__((address_space(3)))
#define GAS __attribute__((address_space(1)))
#define DI __device__ __forceinline__
#define SB() __builtin_amdgcn_sched_barrier(0)
#define KILL() asm volatile("" : "=v"(acc))
#define NOUNROLL _Pragma("clang loop unroll(disable)")

constexpr u32 OFF_FLAG = 4096;
constexpr u32 OFF_DBG = 16384;
constexpr u32 OFF_STATS = 0x20000;
constexpr u32 OFF_Z2T = 0x60000;
constexpr u32 OFF_PKV = OFF_Z2T + 54 * 64 * 1024;
constexpr u32 OFF_PQ = OFF_PKV + 32 * 80 * 2 * 1024;
constexpr u32 OFF_Z1 = OFF_PQ + 32 * 40 * 4 * 512;
constexpr u32 OFF_YO = OFF_Z1 + 32 * 54 * 4 * 512;
constexpr u32 OFF_W3T = OFF_YO + 32 * 47 * 4 * 1024;
constexpr u32 OFF_KVP = OFF_W3T + 40 * 47 * 1024;
constexpr u32 OFF_KVF = OFF_KVP + 8 * 32 * 18 * 1024;
constexpr u32 OFF_FILL = OFF_KVF + 8 * 26 * 1024;
constexpr u32 WS_USED = OFF_FILL + 256 * 1024;
enum { E_Z2 = 0, E_KV = 1, E_O = 2, E_Z1 = 3, E_Y = 4 };
constexpr u32 L_UP = 81920;
constexpr u32 L_PARK = L_UP;
constexpr u32 L_STAT = L_UP + 16384;
constexpr u32 L_NRM = L_UP + 24576;
constexpr u32 L_PART = L_UP + 25600;
constexpr u32 L_SCR = L_UP + 32768;
constexpr u32 L_FL = L_UP + 65536;
constexpr u32 L_PE = 151552;
constexpr u32 L_PL = 153600;
constexpr u32 L_PW = 131072;
constexpr u32 L_OT = 98304;
constexpr u32 L_JOB = 131072;
constexpr u32 L_WX = 102400;
constexpr u32 LDS_BYTES = 163840;
constexpr int SPIN = 1 << 15;
constexpr int PADB = 4, PADA = 0;

DI void glds2(const void* p, u32 v0, u32 v1, u32 lds) {
  asm volatile("s_mov_b32 m0, %3\n\ts_nop 0\n\tglobal_load_lds_dwordx4 %0, %2\n\tglobal_load_lds_dwordx4 %1, %2 offset:1024" ::"v"(v0), "v"(v1), "s"(p), "s"(lds) : "memory");
}
DI void glds4(const void* p, u32 v0, u32 v1, u32 v2, u32 v3, u32 lds) {
  asm volatile("s_mov_b32 m0, %5\n\ts_nop 0\n\tglobal_load_lds_dwordx4 %0, %4\n\tglobal_load_lds_dwordx4 %1, %4 offset:1024\n\t"
               "global_load_lds_dwordx4 %2, %4 offset:2048\n\tglobal_load_lds_dwordx4 %3, %4 offset:3072" ::"v"(v0), "v"(v1), "v"(v2), "v"(v3), "s"(p), "s"(lds) : "memory");
}
template <int N> DI void glds_run(const void* p, u32 v, u32 lds) {
  if (N == 1) asm volatile("s_mov_b32 m0, %2\n\ts_nop 0\n\tglobal_load_lds_dwordx4 %0, %1 sc1" ::"v"(v), "s"(p), "s"(lds) : "memory");
  if (N == 2) asm volatile("s_mov_b32 m0, %2\n\ts_nop 0\n\tglobal_load_lds_dwordx4 %0, %1 sc1\n\tglobal_load_lds_dwordx4 %0, %1 offset:1024 sc1" ::"v"(v), "s"(p), "s"(lds) : "memory");
  if (N == 3) asm volatile("s_mov_b32 m0, %2\n\ts_nop 0\n\tglobal_load_lds_dwordx4 %0, %1 sc1\n\tglobal_load_lds_dwordx4 %0, %1 offset:1024 sc1\n\t"
                           "global_load_lds_dwordx4 %0, %1 offset:2048 sc1" ::"v"(v), "s"(p), "s"(lds) : "memory");
  if (N == 4) asm volatile("s_mov_b32 m0, %2\n\ts_nop 0\n\tglobal_load_lds_dwordx4 %0, %1 sc1\n\tglobal_load_lds_dwordx4 %0, %1 offset:1024 sc1\n\t"
                           "global_load_lds_dwordx4 %0, %1 offset:2048 sc1\n\tglobal_load_lds_dwordx4 %0, %1 offset:3072 sc1" ::"v"(v), "s"(p), "s"(lds) : "memory");
}
template <int OFF> DI v4u gld16(const void* sbase, u32 voff) {
  v4u r;
  asm volatile("global_load_dwordx4 %0, %1, %2 offset:%3" : "=v"(r) : "v"(voff), "s"(sbase), "i"(OFF) : "memory");
  return r;
}
template <int OFF> DI v4u gld16_sc1(const void* sbase, u32 voff) {
  v4u r;
  asm volatile("global_load_dwordx4 %0, %1, %2 offset:%3 sc1" : "=v"(r) : "v"(voff), "s"(sbase), "i"(OFF) : "memory");
  return r;
}
template <int OFF> DI v2u gld8_sc1(const void* sbase, u32 voff) {
  v2u r;
  asm volatile("global_load_dwordx2 %0, %1, %2 offset:%3 sc1" : "=v"(r) : "v"(voff), "s"(sbase), "i"(OFF) : "memory");
  return r;
}
DI u32 gld4_sc1(const void* sbase, u32 voff) {
  u32 r;
  asm volatile("global_load_dword %0, %1, %2 sc1" : "=v"(r) : "v"(voff), "s"(sbase) : "memory");
  return r;
}
DI u32 gld2(const void* sbase, u32 voff) {
  u32 r;
  asm volatile("global_load_ushort %0, %1, %2" : "=v"(r) : "v"(voff), "s"(sbase) : "memory");
  return r;
}
DI void touch(const void* sbase, u32 voff, u32 junk) {
  asm volatile("s_mov_b32 m0, %2\n\ts_nop 4\n\tglobal_load_lds_dword %0, %1" ::"v"(voff), "s"(sbase), "s"(junk) : "memory");
}
DI void st16(v4u v, __amdgpu_buffer_rsrc_t rs, u32 vo, u32 so) {
  __builtin_amdgcn_raw_buffer_store_b128(v, rs, vo, so, 16);
  asm volatile("s_nop 1" ::"v"(v));
}
DI void st16x2(v4u a, v4u b, __amdgpu_buffer_rsrc_t rs, u32 vo, u32 d, u32 so) {
  __builtin_amdgcn_raw_buffer_store_b128(a, rs, vo, so, 16);
  __builtin_amdgcn_raw_buffer_store_b128(b, rs, vo, so + d, 16);
  asm volatile("s_nop 1" ::"v"(a), "v"(b));
}
#define WAIT_BAR(N) asm volatile("s_waitcnt vmcnt(%0) lgkmcnt(0)\n\ts_barrier" ::"i"(N) : "memory")
#define BAR() asm volatile("s_waitcnt lgkmcnt(0)\n\ts_barrier" ::: "memory")
#define BAR0() asm volatile("s_barrier" ::: "memory")
#define MARK()
#define KEEP()
#define DUMP(k, a, b)
#define DBG_ARG
#define DBG_PASS
#define STAMP(j)
#define CSTAMP(j)
template <int N> DI void waitv() { asm volatile("s_waitcnt vmcnt(%0)" ::"i"(N) : "memory"); }
template <int N, class T> DI void waitv(T& a) { asm volatile("s_waitcnt vmcnt(%1)" : "+v"(a) : "i"(N) : "memory"); }
template <int N, class T, class U> DI void waitv(T& a, U& b) { asm volatile("s_waitcnt vmcnt(%2)" : "+v"(a), "+v"(b) : "i"(N) : "memory"); }
DI v4u lds16(u32 a) { return *(const LAS v4u*)(a); }
DI f4v ldsf4(u32 a) { return *(const LAS f4v*)(a); }
DI void sts16(u32 a, v4u v) { *(LAS v4u*)(a) = v; }
DI v2u trd(u32 a) { return __builtin_bit_cast(v2u, __builtin_amdgcn_ds_read_tr16_b64_v4i16((LAS s4v*)(a))); }
DI v4u trd2(u32 a0, u32 a1) { v2u x = trd(a0), y = trd(a1); return v4u{x.x, x.y, y.x, y.y}; }
DI u32 pk2(f2v v) { return __builtin_bit_cast(u32, __builtin_convertvector(v, b2v)); }
DI u32 pk2(float a, float b) { return pk2(f2v{a, b}); }
DI float asf(u32 p) { return __builtin_bit_cast(float, p); }
DI u32 asu(float p) { return __builtin_bit_cast(u32, p); }
DI f2v up2(u32 p) { return f2v{asf(p << 16), asf(p & 0xffff0000u)}; }
DI f2v fma2(f2v a, f2v b, f2v c) { return __builtin_elementwise_fma(a, b, c); }
DI f2v lo2(f4v v) { return __builtin_shufflevector(v, v, 0, 1); }
DI f2v hi2(f4v v) { return __builtin_shufflevector(v, v, 2, 3); }
DI float dot2(u32 a, u32 b, float c) { return __builtin_amdgcn_fdot2_f32_bf16(__builtin_bit_cast(b2v, a), __builtin_bit_cast(b2v, b), c, false); }
DI f16v mfma32(v4u a, v4u b, f16v c) {
 return __builtin_amdgcn_mfma_f32_32x32x16_bf16(__builtin_bit_cast(s8v, a), __builtin_bit_cast(s8v, b), c, 0, 0, 0); }
DI f4v mfma16(v4u a, v4u b, f4v c) { return __builtin_amdgcn_mfma_f32_16x16x32_bf16(__builtin_bit_cast(s8v, a), __builtin_bit_cast(s8v, b), c, 0, 0, 0); }
DI f4v mfma16k(v2u a, v2u b, f4v c) { return __builtin_amdgcn_mfma_f32_16x16x16bf16_1k(__builtin_bit_cast(s4v, a), __builtin_bit_cast(s4v, b), c, 0, 0, 0); }
DI f16v zero16() { f16v z; for (int e = 0; e < 16; ++e) z[e] = 0.f; return z; }
template <int E> DI f2v pair(const f16v& a) { return __builtin_shufflevector(a, a, 2 * E, 2 * E + 1); }
DI float of(float v) { asm("" : "+v"(v)); return v; }
DI u32 pks(float a, float b) { return pk2(of(a), of(b)); }
DI float gelu1(float v) {
  const float a = v * __builtin_fmaf(v * v, -0.10294324f, -2.3022082f);
  return v * __builtin_amdgcn_rcpf(__builtin_amdgcn_exp2f(a) + 1.0f);
}
DI float phi1(float v) {
  return __builtin_amdgcn_fmed3f(__builtin_amdgcn_exp2f(v * 1.44269504f), 0.f, 1.f) + fmaxf(v + 1e-6f, 1e-6f);
}
DI void gelu4(float a, float b, float c, float d, u32& p0, u32& p1) {
  float t0, t1, t2, t3;
  asm("v_mul_f32 %2, %6, %6\n\tv_mul_f32 %3, %7, %7\n\tv_mul_f32 %4, %8, %8\n\tv_mul_f32 %5, %9, %9\n\t"
      "v_fmamk_f32 %2, %2, 0xbdd2d3e8, %10\n\tv_fmamk_f32 %3, %3, 0xbdd2d3e8, %10\n\tv_fmamk_f32 %4, %4, 0xbdd2d3e8, %10\n\tv_fmamk_f32 %5, %5, 0xbdd2d3e8, %10\n\t"
      "v_mul_f32 %2, %6, %2\n\tv_mul_f32 %3, %7, %3\n\tv_mul_f32 %4, %8, %4\n\tv_mul_f32 %5, %9, %5\n\t"
      "v_exp_f32 %2, %2\n\tv_exp_f32 %3, %3\n\tv_exp_f32 %4, %4\n\tv_exp_f32 %5, %5\n\t"
      "v_add_f32 %2, 1.0, %2\n\tv_add_f32 %3, 1.0, %3\n\tv_add_f32 %4, 1.0, %4\n\tv_add_f32 %5, 1.0, %5\n\t"
      "v_rcp_f32 %2, %2\n\tv_rcp_f32 %3, %3\n\tv_rcp_f32 %4, %4\n\tv_rcp_f32 %5, %5\n\t"
      "v_mul_f32 %2, %6, %2\n\tv_mul_f32 %3, %7, %3\n\tv_mul_f32 %4, %8, %4\n\tv_mul_f32 %5, %9, %5\n\t"
      "v_cvt_pk_bf16_f32 %0, %2, %3\n\tv_cvt_pk_bf16_f32 %1, %4, %5"
      : "=&v"(p0), "=&v"(p1), "=&v"(t0), "=&v"(t1), "=&v"(t2), "=&v"(t3) : "v"(a), "v"(b), "v"(c), "v"(d), "v"(-2.3022082f));
}
DI void gelu4s(float a, float b, float c, float d, u32& p0, u32& p1, float& s0, float& s1, float& q0, float& q1) {
  float t0, t1, t2, t3;
  asm("v_mul_f32 %2, %10, %10\n\tv_mul_f32 %3, %11, %11\n\tv_mul_f32 %4, %12, %12\n\tv_mul_f32 %5, %13, %13\n\t"
      "v_fmamk_f32 %2, %2, 0xbdd2d3e8, %14\n\tv_fmamk_f32 %3, %3, 0xbdd2d3e8, %14\n\tv_fmamk_f32 %4, %4, 0xbdd2d3e8, %14\n\tv_fmamk_f32 %5, %5, 0xbdd2d3e8, %14\n\t"
      "v_mul_f32 %2, %10, %2\n\tv_mul_f32 %3, %11, %3\n\tv_mul_f32 %4, %12, %4\n\tv_mul_f32 %5, %13, %5\n\t"
      "v_exp_f32 %2, %2\n\tv_exp_f32 %3, %3\n\tv_exp_f32 %4, %4\n\tv_exp_f32 %5, %5\n\t"
      "v_add_f32 %2, 1.0, %2\n\tv_add_f32 %3, 1.0, %3\n\tv_add_f32 %4, 1.0, %4\n\tv_add_f32 %5, 1.0, %5\n\t"
      "v_rcp_f32 %2, %2\n\tv_rcp_f32 %3, %3\n\tv_rcp_f32 %4, %4\n\tv_rcp_f32 %5, %5\n\t"
      "v_mul_f32 %2, %10, %2\n\tv_mul_f32 %3, %11, %3\n\tv_mul_f32 %4, %12, %4\n\tv_mul_f32 %5, %13, %5\n\t"
      "v_add_f32 %6, %6, %2\n\tv_add_f32 %7, %7, %3\n\tv_fmac_f32 %8, %2, %2\n\tv_fmac_f32 %9, %3, %3\n\t"
      "v_cvt_pk_bf16_f32 %0, %2, %3\n\tv_cvt_pk_bf16_f32 %1, %4, %5\n\t"
      "v_add_f32 %6, %6, %4\n\tv_add_f32 %7, %7, %5\n\tv_fmac_f32 %8, %4, %4\n\tv_fmac_f32 %9, %5, %5"
      : "=&v"(p0), "=&v"(p1), "=&v"(t0), "=&v"(t1), "=&v"(t2), "=&v"(t3), "+v"(s0), "+v"(s1), "+v"(q0), "+v"(q1)
      : "v"(a), "v"(b), "v"(c), "v"(d), "v"(-2.3022082f));
}
DI void phi4(float a, float b, float c, float d, u32& p0, u32& p1) {
  float t0, t1, t2, t3, u0, u1, u2, u3;
  asm("v_mul_f32 %2, 0x3fb8aa3b, %10\n\tv_mul_f32 %3, 0x3fb8aa3b, %11\n\tv_mul_f32 %4, 0x3fb8aa3b, %12\n\tv_mul_f32 %5, 0x3fb8aa3b, %13\n\t"
      "v_exp_f32_e64 %2, %2 clamp\n\tv_add_f32 %6, 0x358637bd, %10\n\tv_exp_f32_e64 %3, %3 clamp\n\tv_add_f32 %7, 0x358637bd, %11\n\t"
      "v_exp_f32_e64 %4, %4 clamp\n\tv_add_f32 %8, 0x358637bd, %12\n\tv_exp_f32_e64 %5, %5 clamp\n\tv_add_f32 %9, 0x358637bd, %13\n\t"
      "v_max_f32 %6, 0x358637bd, %6\n\tv_max_f32 %7, 0x358637bd, %7\n\tv_max_f32 %8, 0x358637bd, %8\n\tv_max_f32 %9, 0x358637bd, %9\n\t"
      "v_add_f32 %2, %2, %6\n\tv_add_f32 %3, %3, %7\n\tv_add_f32 %4, %4, %8\n\tv_add_f32 %5, %5, %9\n\t"
      "v_cvt_pk_bf16_f32 %0, %2, %3\n\tv_cvt_pk_bf16_f32 %1, %4, %5"
      : "=&v"(p0), "=&v"(p1), "=&v"(t0), "=&v"(t1), "=&v"(t2), "=&v"(t3), "=&v"(u0), "=&v"(u1), "=&v"(u2), "=&v"(u3)
      : "v"(a), "v"(b), "v"(c), "v"(d));
}
DI v4u norm8(v4u f, f4v c0, f4v c1, f4v h0, f4v h1) {
  v4u o;
  float t0, t1, t2, t3, t4, t5, t6, t7;
  asm("v_lshlrev_b32 %4, 16, %12\n\tv_and_b32 %5, 0xffff0000, %12\n\tv_lshlrev_b32 %6, 16, %13\n\tv_and_b32 %7, 0xffff0000, %13\n\t"
      "v_lshlrev_b32 %8, 16, %14\n\tv_and_b32 %9, 0xffff0000, %14\n\tv_lshlrev_b32 %10, 16, %15\n\tv_and_b32 %11, 0xffff0000, %15\n\t"
      "v_fma_f32 %4, %4, %16, %24\n\tv_fma_f32 %5, %5, %17, %25\n\tv_fma_f32 %6, %6, %18, %26\n\tv_fma_f32 %7, %7, %19, %27\n\t"
      "v_fma_f32 %8, %8, %20, %28\n\tv_fma_f32 %9, %9, %21, %29\n\tv_fma_f32 %10, %10, %22, %30\n\tv_fma_f32 %11, %11, %23, %31\n\t"
      "v_cvt_pk_bf16_f32 %0, %4, %5\n\tv_cvt_pk_bf16_f32 %1, %6, %7\n\tv_cvt_pk_bf16_f32 %2, %8, %9\n\tv_cvt_pk_bf16_f32 %3, %10, %11"
      : "=&v"(o.x), "=&v"(o.y), "=&v"(o.z), "=&v"(o.w), "=&v"(t0), "=&v"(t1), "=&v"(t2), "=&v"(t3), "=&v"(t4), "=&v"(t5), "=&v"(t6), "=&v"(t7)
      : "v"(f.x), "v"(f.y), "v"(f.z), "v"(f.w), "v"(c0.x), "v"(c0.y), "v"(c0.z), "v"(c0.w), "v"(c1.x), "v"(c1.y), "v"(c1.z), "v"(c1.w),
        "v"(h0.x), "v"(h0.y), "v"(h0.z), "v"(h0.w), "v"(h1.x), "v"(h1.y), "v"(h1.z), "v"(h1.w));
  return o;
}
DI u32 gelu2(f2v v) {
  const f2v a = v * fma2(v * v, f2v{-0.10294324f, -0.10294324f}, f2v{-2.3022082f, -2.3022082f});
  const f2v d = f2v{__builtin_amdgcn_exp2f(a.x), __builtin_amdgcn_exp2f(a.y)} + f2v{1.0f, 1.0f};
  return pk2(v * f2v{__builtin_amdgcn_rcpf(d.x), __builtin_amdgcn_rcpf(d.y)});
}
template <int CTRL> DI float dpp_add(float v) {
  return v + __builtin_bit_cast(float, __builtin_amdgcn_update_dpp(0, __builtin_bit_cast(int, v), CTRL, 0xf, 0xf, true));
}
DI float add32(float v) { auto r = __builtin_amdgcn_permlane32_swap(asu(v), asu(v), false, false); return asf(r[0]) + asf(r[1]); }
DI float add16(float v) { auto r = __builtin_amdgcn_permlane16_swap(asu(v), asu(v), false, false); return asf(r[0]) + asf(r[1]); }
DI int olane() {
  int l;
  asm volatile("v_mbcnt_lo_u32_b32 %0, -1, 0\n\tv_mbcnt_hi_u32_b32 %0, -1, %0" : "=v"(l));
  return l;
}
typedef const u8* cptr;
DI cptr karg(int i) {
  const __attribute__((address_space(4))) u8* ka = (const __attribute__((address_space(4))) u8*)__builtin_amdgcn_kernarg_segment_ptr();
  int o = 8 * i;
  asm volatile("" : "+s"(o));
  return *(const __attribute__((address_space(4))) cptr*)(ka + o);
}
DI u32 ov(u32 v) { asm volatile("" : "+v"(v)); return v; }
DI int osc(int v) { asm volatile("" : "+s"(v)); return v; }
template <class F, int... I> DI void sfor_(F&& f, std::integer_sequence<int, I...>) { (f(std::integral_constant<int, I>{}), ...); }
template <int N, class F> DI void sfor(F&& f) { sfor_(f, std::make_integer_sequence<int, N>{}); }
DI u32 tag_bad(v4u g, u32 t8) { return ((g.x ^ t8) | (g.y ^ t8) | (g.z ^ t8) | (g.w ^ t8)) & 0xffu; }
DI f4v tag_get(v4u v) { return f4v{asf(v.x & ~0xffu), asf(v.y & ~0xffu), asf(v.z & ~0xffu), asf(v.w & ~0xffu)}; }
DI v4u tag_put(f4v c, u32 t8) { return v4u{(asu(c.x) & ~0xffu) | t8, (asu(c.y) & ~0xffu) | t8, (asu(c.z) & ~0xffu) | t8, (asu(c.w) & ~0xffu) | t8}; }

#define RB 4
constexpr int cmin(int a, int b) { return a < b ? a : b; }
constexpr int at_mid(bool job, int i) { return (i + 4 < 8 ? 4 : 0) + (i == 5 || i == 6 ? 3 : 0) + (job ? (i == 0 ? 8 : (i == 4 ? 6 : (i == 5 ? 5 : 0))) : 0); }
constexpr int cnt_B(int NT, bool job, int j) {
  int n = NT * cmin(RB - 1, 15 - j);
  for (int m = (j - RB > 0 ? j - RB : 0); m < j; ++m) if (!(m & 1)) n += at_mid(job, m / 2);
  return cmin(n, 63);
}
constexpr int cnt_mid(int NT, bool job, int i0, int i1) {
  int n = 0;
  for (int j = 2 * i0 + 1; j <= 2 * i1; ++j) if (j + RB < 16) n += NT;
  for (int i = i0 + 1; i < i1; ++i) n += at_mid(job, i);
  return cmin(n, 63);
}
constexpr u32 slotA(int i) { return (u32)(3 - (i & 3)) * 32768u; }

DI void code_prefetch(const u8* start, u32 size, u32 junk) {
  const u32 o = (u32)(((blockIdx.x >> 3) & 7) * 64 + olane()) * 128;
  touch(start, min(o, size - 4), junk);
  touch(start, min(o + 65536, size - 4), junk);
}
struct Ctx {
  const u8* code;
  u32 csize;
  u8* ws;
  __amdgpu_buffer_rsrc_t wsr;
  u32 tag, t8;
  int w, rg, cb;
};
DI void post(const Ctx& c, int ev) {
  const u32 so = OFF_FLAG + ev * 1024 + (osc(c.rg) * 8 + c.cb) * 4;
  const u32 tg = osc(c.tag);
  if (olane() == 0) __builtin_amdgcn_raw_buffer_store_b32(tg, c.wsr, 0, so, 16);
}
DI bool all_eq(v4u f, u32 t) { u32 d = ((f.x ^ t) | (f.y ^ t)) | ((f.z ^ t) | (f.w ^ t)); asm volatile("" : "+v"(d)); return __builtin_amdgcn_ballot_w64(d != 0) == 0; }
DI void spin_all(const Ctx& c, int ev) {
  NOUNROLL for (int s = 0; s < SPIN; ++s) {
    if (all_eq(__builtin_amdgcn_raw_buffer_load_b128(c.wsr, olane() * 16, OFF_FLAG + ev * 1024, 16), c.tag)) break;
    __builtin_amdgcn_s_sleep(1);
  }
}
DI void spin_rg(const Ctx& c, int ev) {
  NOUNROLL for (int s = 0; s < SPIN; ++s) {
    if (__builtin_amdgcn_ballot_w64(__builtin_amdgcn_raw_buffer_load_b32(c.wsr, (olane() & 7) * 4, OFF_FLAG + ev * 1024 + c.rg * 32, 16) != c.tag) == 0) break;
    __builtin_amdgcn_s_sleep(1);
  }
}
DI u32 poll_rg(const Ctx& c, int ev) { return gld4_sc1(c.ws + OFF_FLAG + ev * 1024 + c.rg * 32, (olane() & 7) * 4); }
DI void poll_lds(const Ctx& c, int ev, u32 dst) {
  asm volatile("s_mov_b32 m0, %2\n\ts_nop 0\n\tglobal_load_lds_dword %0, %1 sc1" ::"v"((olane() & 7) * 4), "s"(c.ws + OFF_FLAG + ev * 1024 + c.rg * 32), "s"(dst) : "memory");
}
DI bool polled_lds(const Ctx& c, u32 src) { return __builtin_amdgcn_ballot_w64(*(const LAS u32*)(src + olane() * 4) != c.tag) == 0; }
DI void swap_tile(const u32 (&p)[8], v4u& o0, v4u& o1) {
  auto a = __builtin_amdgcn_permlane32_swap(p[0], p[2], false, false);
  auto c = __builtin_amdgcn_permlane32_swap(p[1], p[3], false, false);
  auto d = __builtin_amdgcn_permlane32_swap(p[4], p[6], false, false);
  auto e = __builtin_amdgcn_permlane32_swap(p[5], p[7], false, false);
  o0 = v4u{(u32)a[0], (u32)c[0], (u32)a[1], (u32)c[1]};
  o1 = v4u{(u32)d[0], (u32)e[0], (u32)d[1], (u32)e[1]};
}
DI v4u pair_tiles(v2u a, v2u b) {
  auto x = __builtin_amdgcn_permlane16_swap(a.x, b.x, false, false);
  auto y = __builtin_amdgcn_permlane16_swap(a.y, b.y, false, false);
  return v4u{(u32)x[0], (u32)y[0], (u32)x[1], (u32)y[1]};
}
DI u32 f_trb(int l) {
  const int q = (l & 15) >> 2, p = l & 3;
  return (8 * (l >> 4) + q) * 64 + (((p >> 1) ^ (q >> 1)) * 16) + (p & 1) * 8;
}
DI u32 f_vn(int l) { return ((l >> 4) & 1) * 512 + (l >> 5) * 256 + (l & 15) * 16; }

template <int NT, bool job> DI void phase2(const Ctx& c, bool late, const f16v& acc1, u32 so_last, u64 epoch DBG_ARG) {
  u8* const ws = c.ws;
  const int w = c.w, rg = c.rg, cb = c.cb, q = w & 3, g = w >> 2;
  const int lane = olane();
  const int ft0 = (cb < 7 ? 7 * cb : 48) + (g ? (cb < 6 ? 4 : 3) : 0);
  const u8* pz = ws + OFF_Z2T + (size_t)ft0 * 1024;
  const int ch1 = q < 2 ? 9 + q : 2 + q, ch2 = q == 0 ? 15 : 5 + q;
  const u32 vl = lane * 16;
  v4u zb[RB][NT];
  auto loadZ = [&](int j) {
    const int i = j >> 1, ch = i == 1 ? ch1 : (i == 2 ? ch2 : (i == 3 ? 11 + q : 4 * i + q));
    const u8* p = pz + (size_t)(2 * ch + (j & 1)) * 54 * 1024;
    zb[j % RB][0] = gld16_sc1<0>(p, vl); zb[j % RB][1] = gld16_sc1<1024>(p, vl); zb[j % RB][2] = gld16_sc1<2048>(p, vl);
    if (NT == 4) zb[j % RB][NT - 1] = gld16_sc1<3072>(p, vl);
  };
#pragma unroll
  for (int j = 0; j < RB; ++j) loadZ(j);
  SB();
  if (late) {
    u32 p[8]; v4u o0, o1;
    sfor<8>([&](auto e) { p[e.value] = gelu2(pair<e.value>(acc1)); });
    swap_tile(p, o0, o1);
    st16x2(o0, o1, c.wsr, f_vn(lane), 2048, so_last);
    if (cb >= 6) BAR0();
  }
  const int t16 = lane & 15, g4 = lane >> 4;
  u32 ra[4];
#pragma unroll
  for (int u = 0; u < 4; ++u) ra[u] = q * 8192 + (u ^ q) * 2048 + t16 * 128 + ((g4 ^ (t16 >> 1)) * 16);
  const u8* pA = karg(4) + (size_t)(64 * rg) * 4096 + (16 + (w >> 1)) * 128;
  const u32 va0 = (32 * (w & 1) + (lane >> 3)) * 4096 + (((lane & 7) ^ g4) * 16), va1 = (va0 ^ 64) + 31744;
  const u32 va2 = va0 + 2 * 31744, va3 = va1 + 2 * 31744;
  STAMP(6);
  BAR();
  if (blockIdx.x == 0 && w == 2 && lane == 0) __hip_atomic_store((u64*)ws, epoch, __ATOMIC_RELAXED, __HIP_MEMORY_SCOPE_AGENT);
  STAMP(7);
  const int kpr = rg < 13 ? rg : (rg < 26 ? rg - 13 : 0);
  const int kti = kpr < 5 ? kpr : (kpr < 9 ? kpr - 5 : kpr - 8), ktj = kpr < 5 ? kpr : (kpr < 9 ? kpr - 4 : kpr - 9);
  const u32 kso = OFF_KVP + (cb * 18 + (rg < 13 ? rg : 13 + kti)) * 32768;
  const int x0 = q < 2 ? 7 * q : 6 * q + 2;
  auto fetchO = [&]() {
    const u8* p = ws + OFF_KVF + (size_t)(cb * 26 + x0) * 1024;
    glds_run<4>(p, vl, L_OT + x0 * 1024);
    if (q < 2) glds_run<3>(p + 4096, vl, L_OT + x0 * 1024 + 4096); else glds_run<2>(p + 4096, vl, L_OT + x0 * 1024 + 4096);
  };
  auto fetchW = [&](int qq) {
    const u8* p = ws + OFF_W3T + (size_t)(cb * 47 + 5 * qq) * 5120 + q * 4096;
    if (q < 3) glds_run<4>(p, vl, L_WX + qq * 15360 + q * 4096); else glds_run<3>(p, vl, L_WX + qq * 15360 + q * 4096);
  };
  f4v acc[NT][4];
  v4u af[2][4];
  u32 bsv = 0;
  v2u pqf[5] = {{0, 0}, {0, 0}, {0, 0}, {0, 0}, {0, 0}}, z1v[NT];
#pragma unroll
  for (int u = 0; u < 4; ++u) af[0][u] = lds16(slotA(0) + ra[u]);
  sfor<16>([&](auto jc) {
    constexpr int j = jc.value, i = j >> 1;
    if (j < 15) {
#pragma unroll
      for (int u = 0; u < 4; ++u) af[(j + 1) & 1][u] = (j & 1) ? lds16(slotA(i + 1) + ra[u]) : lds16(slotA(i) + (ra[u] ^ 64));
    }
    if (NT == 4) asm volatile("s_waitcnt vmcnt(%4)" : "+v"(zb[j % RB][0]), "+v"(zb[j % RB][1]), "+v"(zb[j % RB][2]), "+v"(zb[j % RB][NT - 1]) : "i"(cnt_B(NT, job, j)) : "memory");
    else asm volatile("s_waitcnt vmcnt(%3)" : "+v"(zb[j % RB][0]), "+v"(zb[j % RB][1]), "+v"(zb[j % RB][2]) : "i"(cnt_B(NT, job, j)) : "memory");
    SB();
#pragma unroll
    for (int n = 0; n < NT; ++n)
#pragma unroll
      for (int u = 0; u < 4; ++u)
        acc[n][u] = mfma16(zb[j % RB][n], af[j & 1][u], j == 0 ? f4v{0.f, 0.f, 0.f, 0.f} : acc[n][u]);
    SB();
    if (j + RB < 16) loadZ(j + RB);
    if (j == 13) {
      const u8* p = ws + OFF_Z1 + (size_t)((rg * 54 + ft0) * 4 + q) * 512;
      const u32 vo = (g4 >> 1) * 256 + t16 * 16 + (g4 & 1) * 8;
      z1v[0] = gld8_sc1<0>(p, vo); z1v[1] = gld8_sc1<2048>(p, vo); z1v[2] = gld8_sc1<0>(p + 4096, vo);
      if (NT == 4) z1v[NT - 1] = gld8_sc1<2048>(p + 4096, vo);
      bsv = gld2(karg(5) + (64 * rg + 16 * q) * 2, t16 * 2);
    }
    if (!(j & 1) && i < 7) {
      if (i == 2) waitv<cnt_mid(NT, job, 0, 2)>();
      if (i == 4) waitv<cnt_mid(NT, job, 1, 4)>();
      if (i == 5) waitv<cnt_mid(NT, job, 3, 5)>();
      if (i == 6 && job) {
        asm volatile("s_waitcnt vmcnt(%5)" : "+v"(pqf[0]), "+v"(pqf[1]), "+v"(pqf[2]), "+v"(pqf[3]), "+v"(pqf[4]) : "i"(cnt_mid(NT, job, 5, 6)) : "memory");
        const u32 a = ov(L_OT + x0 * 1024 + vl), a6 = q < 2 ? 6144 : 0;
        v4u v[7];
        NOUNROLL for (int sp = 0; sp < SPIN; ++sp) {
          u32 bad = 0;
#pragma unroll
          for (int k = 0; k < 7; ++k) v[k] = lds16(k < 6 ? a + k * 1024 : a + a6);
#pragma unroll
          for (int k = 0; k < 7; ++k) bad |= (v[k].y ^ c.tag) | (v[k].w ^ c.tag);
          asm volatile("" : "+v"(bad));
          if (__builtin_amdgcn_ballot_w64(bad != 0) == 0) break;
          __builtin_amdgcn_s_sleep(1);
          fetchO();
          waitv<0>();
        }
        const u32 d = ov(slotA(5) + x0 * 512 + vl / 2);
#pragma unroll
        for (int k = 0; k < 7; ++k) *(LAS v2u*)(k < 6 ? d + k * 512 : d + a6 / 2) = v2u{v[k].x, v[k].z};
      }
      BAR();
      if (i + 4 < 8) { glds4(pA, va0, va1, va2, va3, slotA(i) + w * 4096); pA += 512; }
      if (i == 0 && job) {
        glds_run<4>(ws + kso + q * 8192, vl, L_JOB + q * 8192);
        glds_run<4>(ws + kso + q * 8192 + 4096, vl, L_JOB + q * 8192 + 4096);
      }
      if (i == 2) {
        if (w == 1) post(c, E_Z1);
        if (job && rg < 26) {
          f4v s = {0.f, 0.f, 0.f, 0.f};
          u32 bad = 0;
          const int he = ((16 * ktj + t16) * 205) >> 12, hd = ((16 * kti + 4 * q) * 205) >> 12;
          const u32 live = he == hd ? ~0u : 0u;
          const u32 vo = g4 * 1024 + (16 * q + (rg < 13 ? t16 : 0)) * 16, ja = ov(L_JOB + vo);
          auto take = [&](v4u v) {
            bad |= tag_bad(v, c.t8) & live;
            const u32 m = live & ~0xffu;
            s = f4v{of(s.x + asf(v.x & m)), of(s.y + asf(v.y & m)), of(s.z + asf(v.z & m)), of(s.w + asf(v.w & m))};
          };
#pragma unroll
          for (int k = 0; k < 8; ++k) take(lds16(ja + k * 4096));
          if (__builtin_amdgcn_ballot_w64(bad != 0) != 0) {
            NOUNROLL for (int sp = 0; sp < SPIN; ++sp) {
              s = f4v{0.f, 0.f, 0.f, 0.f}; bad = 0;
#pragma unroll
              for (int k = 0; k < 8; ++k) take(__builtin_amdgcn_raw_buffer_load_b128(c.wsr, vo + k * 4096, kso, 16));
              if (__builtin_amdgcn_ballot_w64(bad != 0) == 0) break;
              __builtin_amdgcn_s_sleep(1);
            }
          }
          const v4u o = {pks(add32(add16(s.x)), add32(add16(s.y))), c.tag, pks(add32(add16(s.z)), add32(add16(s.w))), c.tag};
          if (lane < 16) st16(o, c.wsr, vl, OFF_KVF + (cb * 26 + rg) * 1024 + q * 256);
        }
      }
      if (i == 3 && w == 2) poll_lds(c, E_Z1, L_PW);
      if (i == 4 && job) fetchO();
      if (i == 5) {
        if (!polled_lds(c, L_PW)) spin_rg(c, E_Z1);
        fetchW(2 + g);
        if (job) {
          const u8* pp = ws + OFF_PQ + (size_t)((rg * 40 + 5 * cb) * 4 + q) * 512;
          const u32 vo = (g4 >> 1) * 256 + t16 * 16 + (g4 & 1) * 8;
          pqf[0] = gld8_sc1<0>(pp, vo); pqf[1] = gld8_sc1<2048>(pp, vo);
          pqf[2] = gld8_sc1<0>(pp + 4096, vo); pqf[3] = gld8_sc1<2048>(pp + 4096, vo); pqf[4] = gld8_sc1<0>(pp + 8192, vo);
        }
      }
      if (i == 6) {
        fetchW(g);
        if (job) {
          const u32 a = ov(slotA(5) + vl / 2);
          v2u tl[26];
#pragma unroll
          for (int x = 0; x < 26; ++x) tl[x] = *(const LAS v2u*)(a + x * 512);
          f4v num[5], den[5];
#pragma unroll
          for (int tj = 0; tj < 5; ++tj) {
            num[tj] = mfma16k(tl[tj], pqf[tj], f4v{0.f, 0.f, 0.f, 0.f});
            den[tj] = mfma16k(tl[13 + tj], pqf[tj], f4v{1e-6f, 1e-6f, 1e-6f, 1e-6f});
          }
#pragma unroll
          for (int tj = 0; tj < 4; ++tj) {
            num[tj + 1] = mfma16k(tl[5 + tj], pqf[tj], num[tj + 1]); den[tj + 1] = mfma16k(tl[18 + tj], pqf[tj], den[tj + 1]);
          }
#pragma unroll
          for (int tj = 0; tj < 4; ++tj) {
            num[tj] = mfma16k(tl[9 + tj], pqf[tj + 1], num[tj]); den[tj] = mfma16k(tl[22 + tj], pqf[tj + 1], den[tj]);
          }
          const u32 so = OFF_YO + rg * 47 * 4096 + q * 1024 + 5 * cb * 2048 - (cb & 1) * 1536;
          v2u o[5];
#pragma unroll
          for (int tj = 0; tj < 5; ++tj)
            o[tj] = v2u{pks(num[tj].x * __builtin_amdgcn_rcpf(den[tj].x), num[tj].y * __builtin_amdgcn_rcpf(den[tj].y)),
                        pks(num[tj].z * __builtin_amdgcn_rcpf(den[tj].z), num[tj].w * __builtin_amdgcn_rcpf(den[tj].w))};
          const u32 vp = (g4 >> 1) * 256 + t16 * 16 + (g4 & 1) * ((cb & 1) ? 3584 : 512);
          st16(pair_tiles(o[0], o[1]), c.wsr, vp, so);
          st16(pair_tiles(o[2], o[3]), c.wsr, vp, so + 4096);
          __builtin_amdgcn_raw_buffer_store_b64(o[4], c.wsr, (g4 >> 1) * 256 + t16 * 16 + (g4 & 1) * 8, so + 8192, 16);
        }
      }
    }
  });
  STAMP(8); MARK();
  if (NT == 4) asm volatile("s_waitcnt vmcnt(0)" : "+v"(z1v[0]), "+v"(z1v[1]), "+v"(z1v[2]), "+v"(z1v[NT - 1]), "+v"(bsv) :: "memory");
  else asm volatile("s_waitcnt vmcnt(0)" : "+v"(z1v[0]), "+v"(z1v[1]), "+v"(z1v[2]), "+v"(bsv) :: "memory");
  BAR();
  const u32 rb = (g ? (cb < 6 ? 4 : 3) : 0) * 12288 + vl;
  if (w == 1) post(c, E_O);
#pragma unroll
  for (int u = 1; u < 4; ++u) {
    const u32 a = ov(rb + ((u ^ q) * 3 + u - 1) * 1024);
#pragma unroll
    for (int n = 0; n < NT; ++n) *(LAS f4v*)(a + n * 12288) = acc[n][u];
  }
  BAR();
  {
    const float bb = asf(bsv << 16);
    const f4v b4 = {bb, bb, bb, bb};
    const u32 ra0 = ov(rb + q * 3072);
    const u32 so = OFF_YO + (rg * 47 + 20) * 4096 + q * 1024 + ft0 * 2048 - (ft0 & 1) * 1536;
    v2u y[NT];
#pragma unroll
    for (int n = 0; n < NT; ++n) {
      const u32 a = ra0 + n * 12288;
      const f4v s = ((acc[n][0] + b4) + ldsf4(a)) + (ldsf4(a + 1024) + ldsf4(a + 2048));
      y[n] = v2u{pk2(up2(z1v[n].x) * lo2(s)), pk2(up2(z1v[n].y) * hi2(s))};
    }
    const u32 vp = (g4 >> 1) * 256 + t16 * 16 + (g4 & 1) * ((ft0 & 1) ? 3584 : 512);
    st16(pair_tiles(y[0], y[1]), c.wsr, vp, so);
    if (NT == 4) st16(pair_tiles(y[2], y[NT - 1]), c.wsr, vp, so + 4096);
    else __builtin_amdgcn_raw_buffer_store_b64(y[2], c.wsr, (g4 >> 1) * 256 + t16 * 16 + (g4 & 1) * 8, so + 4096, 16);
  }
}

DI void phase3(const Ctx& c DBG_ARG) {
  u8* const ws = c.ws;
  const int w = c.w, rg = c.rg, cb = c.cb, q = w & 3, g = w >> 2;
  const int lane = olane();
  const u32 vl = lane * 16;
  const u8* pwo = ws + OFF_W3T + (size_t)(cb * 47 + 5 * q) * 5120;
  const u8* pwy = ws + OFF_W3T + (size_t)(cb * 47 + 20 + 7 * q) * 5120;
  BAR();
  u32 fl = 0;
  if (w == 0) fl = poll_rg(c, E_O);
  {
    const u32 d = q * 25600;
    if (g == 0) {
      glds_run<4>(pwo + 15360, vl, d); glds_run<4>(pwo + 19456, vl, d + 4096); glds_run<2>(pwo + 23552, vl, d + 8192); glds_run<3>(pwy, vl, d + 10240);
    } else {
      glds_run<4>(pwy + 3072, vl, d + 13312); glds_run<4>(pwy + 7168, vl, d + 17408); glds_run<4>(pwy + 11264, vl, d + 21504);
    }
  }
  const int tid = w * 64 + lane;
  const int t0 = (tid * 6554) >> 16, c0 = tid - t0 * 10, u1 = w < 2 ? tid + 512 : tid, t1 = (u1 * 6554) >> 16, c1 = u1 - t1 * 10;
  const u32 go0 = t0 * 1280 + c0 * 16, go1 = t1 * 1280 + c1 * 16;
  const u8* px = karg(0) + (size_t)(64 * rg) * 1280 + cb * 160;
  const u32 a0 = ov(((c0 >> 1) * 4 + (t0 >> 4)) * 1024 + (c0 & 1) * 512 + ((t0 + 3 * c0) & 15) * 16);
  const u32 a1 = ov(((c1 >> 1) * 4 + (t1 >> 4)) * 1024 + (c1 & 1) * 512 + ((t1 + 3 * c1) & 15) * 16);
  u8* const po = (u8*)karg(7) + (size_t)(64 * rg) * 1280 + cb * 160;
  waitv<12>(fl);
  if (w == 0 && __builtin_amdgcn_ballot_w64(fl != c.tag) != 0) spin_rg(c, E_O);
  BAR0();
  if (w == 1) post(c, E_Y);
  STAMP(9);
  const u8* pyo = ws + OFF_YO + (size_t)(rg * 47 + 5 * q) * 4096 + g * 2048;
  const u8* pyy = ws + OFF_YO + (size_t)(rg * 47 + 20 + 7 * q) * 4096 + g * 2048;
  v4u ya[12][2];
  auto loadY = [&](int s) {
    const u8* p = s < 5 ? pyo + s * 4096 : pyy + (size_t)(s == 11 && q == 3 ? 0 : s - 5) * 4096;
    ya[s][0] = gld16_sc1<0>(p, vl); ya[s][1] = gld16_sc1<1024>(p, vl);
  };
  loadY(0); loadY(1); loadY(2); loadY(3); loadY(4);
  u32 fy = 0;
  if (w == 0) fy = poll_rg(c, E_Y);
  v4u xv0, xv1;
  f4v oc[5][2];
  v4u wr[2][5];
  const u32 rx = ov(L_WX + q * 15360 + vl), ry = ov(q * 25600 + vl);
  auto wrow = [&](int s) -> u32 { return s < 3 ? rx + s * 5120 : (s < 8 ? ry + (s - 3) * 5120 : (s < 11 ? rx + (s - 8) * 5120 : ry)); };
#pragma unroll
  for (int n = 0; n < 5; ++n) wr[0][n] = lds16(wrow(0) + n * 1024);
  sfor<12>([&](auto sc) {
    constexpr int s = sc.value;
    constexpr int cnt[12] = {8, 6, 17, 15, 15, 15, 15, 63, 6, 4, 0, 0};
    if (s == 2) {
      if (w == 0) {
        waitv<0>(fy);
        if (__builtin_amdgcn_ballot_w64(fy != c.tag) != 0) spin_rg(c, E_Y);
      }
      BAR();
      STAMP(10);
      loadY(5); loadY(6); loadY(7);
      const u8* p = pwy + 15360 + g * 8192;
      const u32 d = L_WX + q * 15360 + g * 8192;
      glds_run<4>(p, vl, d);
      if (g == 0) glds_run<4>(p + 4096, vl, d + 4096); else glds_run<3>(p + 4096, vl, d + 4096);
    }
    if (s == 7) {
      WAIT_BAR(8);
      if (q < 3) {
        if (g == 0) glds_run<2>(pwy + 30720, vl, q * 25600); else glds_run<3>(pwy + 32768, vl, q * 25600 + 2048);
      }
      xv0 = gld16<0>(px, go0); xv1 = gld16<0>(px, go1);
    }
    if (s == 10) { waitv<0>(xv0, xv1); BAR(); }
    if (s < 11) {
#pragma unroll
      for (int n = 0; n < 5; ++n) wr[(s + 1) & 1][n] = lds16(wrow(s + 1) + n * 1024);
    }
    waitv<cnt[s] + (s == 8 || s == 9 ? 2 : 0)>(ya[s][0], ya[s][1]);
    if (s == 0 || s == 5) MARK();
    SB();
    if (s < 11 || q < 3) {
#pragma unroll
      for (int n = 0; n < 5; ++n)
#pragma unroll
        for (int tt = 0; tt < 2; ++tt) oc[n][tt] = mfma16(wr[s & 1][n], ya[s][tt], s == 0 ? f4v{0.f, 0.f, 0.f, 0.f} : oc[n][tt]);
    }
    SB();
    if (s >= 3 && s <= 6) loadY(s + 5);
  });
  u32 rr[5];
#pragma unroll
  for (int n = 0; n < 5; ++n) rr[n] = ov(q * 20480 + g * 2048 + n * 4096 + (lane >> 4) * 256 + ((lane + 6 * n + 3 * (lane >> 5)) & 15) * 16);
  STAMP(11);
  BAR();
#pragma unroll
  for (int n = 0; n < 5; ++n)
#pragma unroll
    for (int tt = 0; tt < 2; ++tt) *(LAS f4v*)(rr[n] + tt * 1024) = oc[n][tt];
  BAR();
  auto finish = [&](u32 a, u32 go, v4u xv) {
    const f4v lo = (ldsf4(a) + ldsf4(a + 20480)) + (ldsf4(a + 40960) + ldsf4(a + 61440));
    const f4v hi = (ldsf4(a + 256) + ldsf4(a + 20480 + 256)) + (ldsf4(a + 40960 + 256) + ldsf4(a + 61440 + 256));
    const v4u o = {pk2(up2(xv.x) + lo2(lo)), pk2(up2(xv.y) + hi2(lo)), pk2(up2(xv.z) + lo2(hi)), pk2(up2(xv.w) + hi2(hi))};
    *(GAS v4u*)(po + go) = o;
  };
  finish(a0, go0, xv0);
  if (w < 2) finish(a1, go1, xv1);
  STAMP(12); CSTAMP(31); MARK(); MARK();
}

__global__ __launch_bounds__(512, 2) void fused(const u8* __restrict__ x, const u8* __restrict__ wqkv, const u8* __restrict__ wo,
                                                const u8* __restrict__ win, const u8* __restrict__ wsp, const u8* __restrict__ bsp,
                                                const u8* __restrict__ wout, u8* __restrict__ out, u8* ws) {
  extern __shared__ __attribute__((aligned(16))) u8 smem[];
  const int lane = threadIdx.x & 63;
  const int w = __builtin_amdgcn_readfirstlane(threadIdx.x >> 6);
  const int b = blockIdx.x, xg = b & 7, jb = b >> 3;
  const int rg = 8 * (xg >> 1) + (jb & 7), cb = 4 * (xg & 1) + (jb >> 3);
  const int g = w >> 2, wq4 = w & 3, mh = w & 1, hf = (w >> 1) & 1;
  Ctx c;
  c.ws = ws; c.w = w; c.rg = rg; c.cb = cb;
  c.wsr = __builtin_amdgcn_make_buffer_rsrc(ws, 0, WS_USED, 0x00020000);
  const auto wsr = c.wsr;
  asm volatile("s_getpc_b64 %0\n.Lk_begin:\n\ts_mov_b32 %1, .Lk_end-.Lk_begin" : "=s"(c.code), "=s"(c.csize));
  MARK();
  STAMP(0); CSTAMP(30);
  const int r8 = lane >> 3, s8 = lane & 7;
  v4u xr[10];
  {
    const u8* xb = x + (size_t)(64 * rg + 8 * w) * 1280;
    const u32 vo = r8 * 1280 + s8 * 16;
    xr[0] = gld16<0>(xb, vo); xr[1] = gld16<128>(xb, vo); xr[2] = gld16<256>(xb, vo); xr[3] = gld16<384>(xb, vo); xr[4] = gld16<512>(xb, vo);
    xr[5] = gld16<640>(xb, vo); xr[6] = gld16<768>(xb, vo); xr[7] = gld16<896>(xb, vo); xr[8] = gld16<1024>(xb, vo); xr[9] = gld16<1152>(xb, vo);
  }
  v2u ep = gld8_sc1<0>(ws, 0u);
  const u32 blksA = (u32)(43 + cb) | (u32)(12 + cb) << 8 | (u32)(cb + (cb < 2 ? 28 : -2)) << 16 | (u32)(34 + cb) << 24;
  const u32 blksB = (u32)(cb + (cb < 6 ? 51 : 4)) | (u32)(20 + cb) << 8 | (u32)(cb + (cb < 4 ? 6 : 26)) << 16 | 42u << 24;
  const u32 blks = g ? blksB : blksA;
  const int nr = (g == 0 || cb == 7) ? 4 : 3;
  const bool late = (g == 0) != (cb == 7);
  const u8* const pAu = wsp + (size_t)(64 * rg) * 4096 + (g ? 6 : 11) * 128;
  const u8* const winb = win - 30 * 128;
  const u8 *pw, *pwn;
  u32 stw, stwn, vo0, vo1, vo0n, vo1n;
  auto next_desc = [&](int r) {
    const int l = olane();
    const bool isA = r >= nr;
    const u32 bk = ((u32)osc(blks) >> ((8 * r) & 31)) & 0xff;
    const bool isq = bk < 30;
    const u8* pb = isq ? wqkv : winb;
    pwn = isA ? pAu : pb + bk * 128;
    const u32 d = isA ? 4096u : (isq ? 3840u : 3456u);
    stwn = isA ? 128u : 64 * d;
    const u32 xx = (u32)(l >> 4), m = (xx & (isA ? 3u : 0u)) | ((xx << 2) & (isA ? 0u : 4u));
    vo0n = (16 * wq4 + (l >> 3)) * d + (((l & 7) ^ m) * 16);
    vo1n = (vo0n ^ (isA ? 64u : 0u)) + 8 * d - 1024;
  };
  auto touch_blk = [&](u32 bk, u32 junk, bool tail = false) {
    const bool isq = bk < 30;
    touch((isq ? wqkv : winb) + bk * 128, (u32)(tail ? 320 + 40 * (jb & 7) + 10 * wq4 + min(olane(), 9) : 80 * (jb & 7) + 20 * wq4 + min(olane(), 19)) * (isq ? 3840u : 3456u), junk);
  };
  next_desc(0);
  pw = pwn; stw = stwn; vo0 = vo0n; vo1 = vo1n;
  next_desc(1);
  const u32 ring = g * 40960;
  const u32 wl = ring + wq4 * 2048;
  asm volatile("s_waitcnt vmcnt(0)"
               : "+v"(xr[0]), "+v"(xr[1]), "+v"(xr[2]), "+v"(xr[3]), "+v"(xr[4]), "+v"(xr[5]), "+v"(xr[6]), "+v"(xr[7]), "+v"(xr[8]), "+v"(xr[9]), "+v"(ep)
               :: "memory");
  STAMP(1); MARK();
  const u64 epoch = (((u64)(u32)__builtin_amdgcn_readfirstlane(ep.y) << 32) | (u32)__builtin_amdgcn_readfirstlane(ep.x)) + 1;
  c.t8 = (u32)epoch & 0xffu;
  c.tag = (u32)epoch * 0x9E3779B1u ^ (u32)(epoch >> 32);
  {
    float a1[4] = {0.f, 0.f, 0.f, 0.f}, a2[4] = {0.f, 0.f, 0.f, 0.f};
#pragma unroll
    for (int j = 0; j < 10; ++j) {
#pragma unroll
      for (int e = 0; e < 4; ++e) a1[e] = dot2(xr[j][e], 0x3f803f80u, a1[e]);
#pragma unroll
      for (int e = 0; e < 4; ++e) a2[e] = dot2(xr[j][e], xr[j][e], a2[e]);
    }
    float s1 = (a1[0] + a1[1]) + (a1[2] + a1[3]), s2 = (a2[0] + a2[1]) + (a2[2] + a2[3]);
    s1 = dpp_add<0xB1>(s1); s2 = dpp_add<0xB1>(s2); s1 = dpp_add<0x4E>(s1); s2 = dpp_add<0x4E>(s2); s1 = dpp_add<0x141>(s1); s2 = dpp_add<0x141>(s2);
    const float mean = s1 * (1.0f / 640.0f);
    const float rs = __builtin_amdgcn_rsqf(fmaxf(s2 * (1.0f / 640.0f) - mean * mean, 0.f) + 1e-5f), sh = -mean * rs;
    const f2v rs2 = {rs, rs}, sh2 = {sh, sh};
    const int row = 8 * w + r8;
    const u32 rb0 = L_UP + row * 1280 + ((s8 ^ (row & 15)) * 16), rb1 = rb0 ^ 128;
#pragma unroll
    for (int j = 0; j < 10; j += 2) {
      f2v u[8];
#pragma unroll
      for (int e = 0; e < 8; ++e) u[e] = up2(xr[j + (e >> 2)][e & 3]);
#pragma unroll
      for (int e = 0; e < 8; ++e) u[e] = fma2(u[e], rs2, sh2);
      sts16(rb0 + (j >> 1) * 256, v4u{pk2(u[0]), pk2(u[1]), pk2(u[2]), pk2(u[3])});
      sts16(rb1 + (j >> 1) * 256, v4u{pk2(u[4]), pk2(u[5]), pk2(u[6]), pk2(u[7])});
    }
  }
  auto prefill = [&]() {
    touch_blk(blks & 0xff, wl, true); touch_blk((blks >> 8) & 0xff, wl);
#pragma unroll
    for (int i = 0; i < 5; ++i) { glds2(pw, vo0, vo1, wl + i * 8192); pw += stw; }
  };
  if (g == 0) prefill();
  BAR();
  STAMP(2);
  v4u xa[40], wf[2][4];
  const u32 tb = ov(ring + (8 * (lane >> 5) + ((lane & 15) >> 2)) * 128 + ((hf ^ ((lane & 15) >> 3)) * 64) + ((lane >> 4) & 1) * 32 + (lane & 3) * 8);
  auto read_x = [&]() {
    const int row = 32 * mh + (lane & 31);
    const u32 rb = L_UP + row * 1280, X = (u32)((lane >> 5) ^ (row & 15));
#pragma unroll
    for (int ks = 0; ks < 40; ++ks) xa[ks] = lds16(rb + (ks >> 3) * 256 + ((((2 * ks) & 15) ^ X) * 16));
  };
  auto read_w0 = [&]() {
#pragma unroll
    for (int kk = 0; kk < 4; ++kk) wf[0][kk] = trd2(tb + kk * 2048, tb + kk * 2048 + 512);
  };
  f16v acc;
  bool okz = false;
  const u32 vl = ov(lane * 16), vn = ov(f_vn(lane)), trb = ov(L_UP + w * 2048 + f_trb(lane));
  const u32 ib = ov(L_UP + w * 2048 + (lane & 31) * 64 + (((lane >> 5) ^ ((lane >> 1) & 3)) * 16));
  auto block = [&](bool fin) {
    __builtin_amdgcn_s_setprio(1);
#pragma unroll
    for (int i = 0; i < 10; ++i) {
      if (i < 4) WAIT_BAR(8); else WAIT_BAR(6);
      const u32 ra = tb + ((i + 1) % 5) * 8192;
      SB();
      acc = mfma32(wf[i & 1][0], xa[4 * i], i == 0 ? zero16() : acc);
      SB();
      wf[(i + 1) & 1][0] = trd2(ra, ra + 512);
      wf[(i + 1) & 1][1] = trd2(ra + 2048, ra + 2048 + 512);
      SB();
      acc = mfma32(wf[i & 1][1], xa[4 * i + 1], acc);
      SB();
      wf[(i + 1) & 1][2] = trd2(ra + 4096, ra + 4096 + 512);
      wf[(i + 1) & 1][3] = trd2(ra + 6144, ra + 6144 + 512);
      SB();
      acc = mfma32(wf[i & 1][2], xa[4 * i + 2], acc);
      SB();
      if (i == 5) { pw = pwn; stw = stwn; vo0 = vo0n; vo1 = vo1n; }
      if (i == 6 && fin && wq4 == 0) glds_run<1>(ws + OFF_FLAG + E_Z2 * 1024, vl, L_PL);
      glds2(pw, vo0, vo1, wl + (i % 5) * 8192);
      pw += stw;
      SB();
      acc = mfma32(wf[i & 1][3], xa[4 * i + 3], acc);
      SB();
    }
    __builtin_amdgcn_s_setprio(0);
    asm volatile("s_nop 7\n\ts_nop 3" ::: "memory");
  };
  auto fill2 = [&]() {
    const u32 so = OFF_FILL + osc(b) * 1024 + w * 128;
    __builtin_amdgcn_raw_buffer_store_b32(vl, wsr, 0, so, 0);
    asm volatile("" ::: "memory");
    __builtin_amdgcn_raw_buffer_store_b32(vl, wsr, 0, so + 64, 0);
    asm volatile("" ::: "memory");
  };
  u32 pk[8];
  v4u o0, o1;
#define SLICE() do { SB(); BAR0(); SB(); } while (0)
#define SLICEW() do { SB(); BAR(); SB(); } while (0)
  float s0, s1, q0, q1;
  auto act = [&](auto kind, auto ec) {
    constexpr int e = ec.value;
    const float a = acc[4 * e], b = acc[4 * e + 1], cc = acc[4 * e + 2], d = acc[4 * e + 3];
    if (kind.value == 0) { pk[2 * e] = pk2(a, b); pk[2 * e + 1] = pk2(cc, d); }
    if (kind.value == 1) phi4(a, b, cc, d, pk[2 * e], pk[2 * e + 1]);
    if (kind.value == 2) gelu4(a, b, cc, d, pk[2 * e], pk[2 * e + 1]);
    if (kind.value == 3) {
      if (e == 0) { s0 = 0.f; s1 = 0.f; q0 = 0.f; q1 = 0.f; }
      gelu4s(a, b, cc, d, pk[2 * e], pk[2 * e + 1], s0, s1, q0, q1);
    }
  };
  std::integral_constant<int, 0> K_NONE, I0; std::integral_constant<int, 1> K_PHI, I1; std::integral_constant<int, 2> K_GELU, I2; std::integral_constant<int, 3> K_Z2, I3;
  auto act4 = [&](auto kind) { SLICE(); act(kind, I0); SLICE(); act(kind, I1); SLICE(); act(kind, I2); SLICE(); act(kind, I3); };
  auto put_sums = [&]() {
    *(LAS f2v*)(L_PART + (w >> 1) * 512 + mh * 256 + (ov(vl) & 496) / 2) = f2v{add32(of(s0 + s1)), add32(of(q0 + q1))};
  };
  auto put_img = [&](u32 off) { swap_tile(pk, o0, o1); sts16(ib + off, o0); sts16((ov(ib) ^ 32) + off, o1); };
  auto get_img = [&]() { o0 = trd2(trb + 32768, trb + 32768 + 256 + 32); o1 = trd2(trb + 32768 + 32, trb + 32768 + 256); };
  auto put_kv = [&](int pc) { st16x2(o0, o1, wsr, vl, 2048, OFF_PKV + (osc(rg) * 160 + 4 * (pc - 20) + mh) * 1024); };
  auto put_q = [&](int pc) { swap_tile(pk, o0, o1); st16x2(o0, o1, wsr, vn, 2048, OFF_PQ + (osc(rg) * 160 + 8 * pc + 2 * mh) * 512); };
  auto put_z1 = [&](int pc) { swap_tile(pk, o0, o1); st16x2(o0, o1, wsr, vn, 2048, OFF_Z1 + (osc(rg) * 216 + 8 * (pc - 60) + 2 * mh) * 512); };
  auto load_kv = [&]() {
    const u8* pk = ws + OFF_PKV + (size_t)(rg * 80 + 5 * cb) * 2048 + (wq4 >> 1) * 81920 + (wq4 & 1) * 5120;
    glds_run<4>(pk, vl, L_JOB + wq4 * 5120); glds_run<1>(pk + 4096, vl, L_JOB + wq4 * 5120 + 4096);
  };
  auto put_kvp = [&]() {
    v4u kv[8];
    const u32 ka = ov(L_JOB + wq4 * 2048 + vl);
#pragma unroll
    for (int k = 0; k < 8; ++k) kv[k] = lds16(ka + (k >> 2) * 10240 + (k & 3) * 1024);
    SB(); MARK();
    const f4v z4 = {0.f, 0.f, 0.f, 0.f};
    const v4u one = {0x3f803f80u, 0x3f803f80u, 0x3f803f80u, 0x3f803f80u};
    const u32 so = OFF_KVP + (cb * 18 * 32 + rg) * 1024 + wq4 * 32768;
    const f4v ta = mfma16(kv[1], kv[7], mfma16(kv[0], kv[6], z4)), tb = mfma16(kv[3], kv[5], mfma16(kv[2], kv[4], z4)),
              tc = mfma16(kv[3], kv[7], mfma16(kv[2], kv[6], z4)), td = mfma16(kv[3], one, mfma16(kv[2], one, z4)),
              te = mfma16(kv[1], kv[5], mfma16(kv[0], kv[4], z4)), tf = mfma16(kv[1], one, mfma16(kv[0], one, z4));
    const int l = olane(), e16 = 16 * wq4 + (l & 15), d16 = 16 * wq4 + 4 * (l >> 4);
    const int he0 = (e16 * 205) >> 12, he1 = ((e16 + 16) * 205) >> 12, hd0 = (d16 * 205) >> 12, hd1 = ((d16 + 16) * 205) >> 12;
    if (hd0 == he1) st16(tag_put(ta, c.t8), wsr, vl, so + 5 * 32768);
    if (hd1 == he0) st16(tag_put(tb, c.t8), wsr, vl, so + 9 * 32768);
    if (hd1 == he1) st16(tag_put(tc, c.t8), wsr, vl, so + 32768);
    if ((l & 15) == 0) st16(tag_put(td, c.t8), wsr, vl, so + 14 * 32768);
    if (wq4 == 0) {
      st16(tag_put(te, c.t8), wsr, vl, so);
      if ((l & 15) == 0) st16(tag_put(tf, c.t8), wsr, vl, so + 13 * 32768);
    }
  };
  auto piece = [&](int r) -> int { return (int)(((u32)osc(blks) >> (8 * r)) & 0xff) * 2 + hf; };
  if (g == 0) {
    read_x();
    WAIT_BAR(8);
    fill2();
    read_w0();
    STAMP(3);
    {
      {
        block(false);
        STAMP(20);
        act4(K_Z2);
        SLICE();
        put_sums();
        if (cb + hf > 0) { put_img(0); fill2(); } else put_z1(86);
        SLICEW();
        next_desc(2);
      }
      {
        block(false);
        STAMP(21);
        SLICE();
        glds_run<2>(ws + OFF_STATS + (osc(rg) * 8 + 2 * wq4) * 1024, vl, L_STAT + osc(wq4) * 2048);
        act(K_PHI, I0); act(K_PHI, I1);
        SLICE();
        act(K_PHI, I2); act(K_PHI, I3);
        SLICE();
        put_img(32768);
        const int l = olane();
        const u32 a = ov(L_FL + (8 * (l >> 4) + ((l & 15) >> 2)) * 256 + wq4 * 64 + (l & 3) * 8);
        const v4u t0 = trd2(a, a + 1024), t1 = trd2(a + 32, a + 1024 + 32);
        SLICE();
        get_img();
        if (b < 235) {
          const int bb = osc(b), ks = (bb * 205) >> 10, ff = bb - ks * 5;
          const int ct = ff * 8 + 2 * wq4, cq0 = (ct * 205) >> 10, cq1 = ((ct + 1) * 205) >> 10;
          st16(t0, wsr, vl, OFF_W3T + (cq0 * 230 + ks * 5 + ct) * 1024);
          st16(t1, wsr, vl, OFF_W3T + (cq1 * 230 + ks * 5 + ct + 1) * 1024);
          waitv<2>();
        } else waitv<0>();
        SLICE();
        put_kv(piece(1));
        next_desc(3);
        SLICE();
        if (w == 0) {
          float s1 = 0.f, s2 = 0.f;
          u32 bad = 0;
          auto take = [&](v4u v0, v4u v1, v4u v2, v4u v3) {
            bad |= ((v0.y ^ c.tag) | (v0.w ^ c.tag) | (v1.y ^ c.tag) | (v1.w ^ c.tag)) | ((v2.y ^ c.tag) | (v2.w ^ c.tag) | (v3.y ^ c.tag) | (v3.w ^ c.tag));
            s1 = of(s1 + of(of(asf(v0.x) + asf(v1.x)) + of(asf(v2.x) + asf(v3.x))));
            s2 = of(s2 + of(of(asf(v0.z) + asf(v1.z)) + of(asf(v2.z) + asf(v3.z))));
          };
          const u32 sa = ov(L_STAT + vl);
          take(lds16(sa), lds16(sa + 1024), lds16(sa + 2048), lds16(sa + 3072));
          SB();
          take(lds16(sa + 4096), lds16(sa + 5120), lds16(sa + 6144), lds16(sa + 7168));
          asm volatile("" : "+v"(bad));
          if (__builtin_amdgcn_ballot_w64(bad != 0) != 0) {
            const u32 so = OFF_STATS + osc(rg) * 8192;
            auto ld = [&](int e) { return __builtin_amdgcn_raw_buffer_load_b128(wsr, vl, so + e * 1024, 16); };
            NOUNROLL for (int sp = 0; sp < SPIN; ++sp) {
              s1 = 0.f; s2 = 0.f; bad = 0;
              take(ld(0), ld(1), ld(2), ld(3));
              SB();
              take(ld(4), ld(5), ld(6), ld(7));
              asm volatile("" : "+v"(bad));
              if (__builtin_amdgcn_ballot_w64(bad != 0) == 0) break;
              __builtin_amdgcn_s_sleep(1);
            }
          }
          const float mean = of(s1 * (1.0f / 864.0f));
          const float rs = __builtin_amdgcn_rsqf(fmaxf(of(s2 * (1.0f / 864.0f)) - mean * mean, 0.f) + 1e-5f);
          const u32 na = ov(L_NRM + ov(vl) / 4);
          *(LAS float*)(na) = rs;
          *(LAS float*)(na + 256) = -mean * rs;
        }
      }
      {
        block(false);
        STAMP(22);
        SLICE();
        if (w == 0) post(c, E_Z2);
        if (cb < 2) {
          act(K_NONE, I0); act(K_NONE, I1); act(K_NONE, I2); act(K_NONE, I3);
          put_img(32768);
          SLICE();
          get_img();
          SLICE();
          put_kv(piece(2));
        } else {
          act(K_PHI, I0); act(K_PHI, I1);
          SLICE();
          act(K_PHI, I2); act(K_PHI, I3);
          SLICE();
          put_q(piece(2));
        }
        const int l = olane();
        const u8* pa = wsp + (size_t)(64 * rg) * 4096;
        const u32 va0 = (16 * wq4 + (l >> 3)) * 4096 + (((l & 7) ^ (l >> 4)) * 16), va1 = (va0 ^ 64) + 31744;
        next_desc(4);
#pragma unroll
        for (int u = 0; u < 6; ++u) {
          if (!(u & 1)) SLICE();
          glds2(pa + (u < 2 ? 4 + u : u - 2) * 128, va0, va1, L_UP + u * 8192 + wq4 * 2048);
        }
      }
      KEEP();
      block(late);
      MARK();
      STAMP(23);
    }
    if (!late) {
      act4(K_GELU);
      SLICE();
      put_z1(piece(3));
      SLICE();
    }
  } else {
    prefill();
    BAR0();
    {
      {
        BAR0();
        read_x();
        BAR0(); BAR0();
        const int bb = b < 235 ? osc(b) : 0, ks = (bb * 205) >> 10, ff = bb - ks * 5, l = olane();
        WAIT_BAR(8);
        read_w0();
        const u8* src = (ks < 20 ? karg(2) : karg(6) - 20 * 32 * 1280) + (size_t)ks * 32 * 1280 + ff * 256;
        const u32 vo = (8 * wq4 + (l >> 4)) * 1280 + (l & 15) * 16;
        glds2(src, vo, vo + 4 * 1280 - 1024, L_FL + osc(wq4) * 2048);
        BAR0(); BAR0();
      }
      {
        block(false);
        if (cb < 6) {
          act4(K_Z2);
          SLICE();
          put_sums();
          put_img(0);
        } else {
          SLICE();
          act(K_PHI, I0); act(K_PHI, I1);
          SLICE();
          act(K_PHI, I2); act(K_PHI, I3);
          SLICE();
          put_img(32768);
          SLICE();
          get_img();
          SLICE();
          put_kv(piece(0));
        }
        SLICEW();
        const u32 a = ov(L_PART + ov(vl) / 2);
        const f2v s0 = *(const LAS f2v*)(a), s1 = *(const LAS f2v*)(a + 512), s2 = *(const LAS f2v*)(a + 1024), s3 = *(const LAS f2v*)(a + 1536);
        SB();
        next_desc(2);
        SB();
        if (w == 4) {
          const f2v z = {0.f, 0.f};
          const f2v sa_ = cb > 0 ? s0 : z, sb_ = cb < 6 ? s2 : z, sc_ = cb < 6 ? s3 : z;
          const f2v s = {of(of(s1.x + sa_.x) + of(sb_.x + sc_.x)), of(of(s1.y + sa_.y) + of(sb_.y + sc_.y))};
          const u32 tg = osc(c.tag);
          st16(v4u{asu(s.x), tg, asu(s.y), tg}, wsr, vl, OFF_STATS + (osc(rg) * 8 + cb) * 1024);
        }
        fill2();
      }
      {
        block(false);
        SLICE();
        const u32 na = ov(L_NRM + mh * 128 + (ov(vl) >> 8) * 32), t = ov(trb - 8192);
        const f4v c0 = ldsf4(na), c1 = ldsf4(na + 16), h0 = ldsf4(na + 256), h1 = ldsf4(na + 272);
        v4u f0 = trd2(t, t + 256 + 32), f1 = trd2(t + 32, t + 256);
        SB();
        act(K_NONE, I0); act(K_NONE, I1); act(K_NONE, I2); act(K_NONE, I3);
        put_img(32768);
        SLICE();
        const u32 so = OFF_Z2T + ((2 * osc(rg) + mh) * 54 + 4 * cb + 2 * hf - 2) * 1024;
        {
          const v4u n0 = norm8(f0, c0, c1, h0, h1), n1 = norm8(f1, c0, c1, h0, h1);
          f0 = trd2(trb, trb + 256 + 32); f1 = trd2(trb + 32, trb + 256);
          if (cb + hf > 0) st16x2(n0, n1, wsr, vl, 1024, so);
        }
        SLICE();
        {
          const v4u n0 = norm8(f0, c0, c1, h0, h1), n1 = norm8(f1, c0, c1, h0, h1);
          get_img();
          if (cb < 6) st16x2(n0, n1, wsr, vl, 1024, so + 32 * 1024);
        }
        SLICE();
        put_kv(piece(1));
        SLICE();
        next_desc(3);
        SLICE();
      }
      {
        block(false);
        if (w == 4) poll_lds(c, E_Z2, L_PE);
        auto kv_early = [&]() {
          if (cb < 6) {
            if (!polled_lds(c, L_PE)) spin_rg(c, E_Z2);
            load_kv();
          }
        };
        if (cb < 4) {
          SLICE();
          act(K_PHI, I0); act(K_PHI, I1);
          SLICE();
          act(K_PHI, I2); act(K_PHI, I3);
          waitv<0>();
          SLICE();
          kv_early();
          SLICE();
          put_q(piece(2));
          SLICE();
        } else {
          SLICE(); act(K_GELU, I0);
          SLICE(); act(K_GELU, I1);
          waitv<0>();
          SLICE(); kv_early(); act(K_GELU, I2);
          SLICE(); act(K_GELU, I3);
          SLICE();
          put_z1(piece(2));
        }
        SB(); WAIT_BAR(2); SB();
        if (w == 4) post(c, E_KV);
      }
    }
    if (late) {
      next_desc(4);
      block(true);
    } else {
      KEEP(); MARK();
      if (cb < 6) put_kvp();
      MARK();
      BAR0(); BAR0(); BAR0(); BAR0();
      MARK();
    }
  }
  if (cb >= 6 && !late) {
    spin_rg(c, E_KV);
    load_kv();
    WAIT_BAR(0);
    put_kvp();
  }
  okz = all_eq(lds16(L_PL + vl), c.tag);
  STAMP(4); MARK();
  if (!okz) spin_all(c, E_Z2);
  STAMP(5); MARK();
  const u32 so_last = OFF_Z1 + (rg * 216 + 8 * ((int)(blks >> 24) * 2 + hf - 60) + 2 * mh) * 512;
  if (g == 1) phase2<3, true>(c, late, acc, so_last, epoch DBG_PASS);
  else if (cb < 6) phase2<4, false>(c, late, acc, so_last, epoch DBG_PASS);
  else phase2<3, false>(c, late, acc, so_last, epoch DBG_PASS);
  phase3(c DBG_PASS);
  asm volatile(".Lk_end:");
}

extern "C" void kernel_launch(void* const* d_in, const int* in_sizes, int n_in, void* d_out, int out_size, void* d_ws, size_t ws_size,
                              hipStream_t stream) {
  (void)in_sizes; (void)n_in; (void)out_size; (void)ws_size;
  (void)hipFuncSetAttribute((const void*)fused, hipFuncAttributeMaxDynamicSharedMemorySize, LDS_BYTES);
  fused<<<256, 512, LDS_BYTES, stream>>>((const u8*)d_in[0], (const u8*)d_in[1], (const u8*)d_in[2], (const u8*)d_in[4], (const u8*)d_in[5],
                                         (const u8*)d_in[6], (const u8*)d_in[7], (u8*)d_out, (u8*)d_ws);
}
```

```cpp
#include <hip/hip_runtime.h>
#include <hip/hip_bf16.h>
#include <utility>

typedef unsigned int u32;
typedef unsigned long long u64;
typedef unsigned char u8;
typedef u32 v4u __attribute__((ext_vector_type(4)));
typedef u32 v2u __attribute__((ext_vector_type(2)));
typedef float f16v __attribute__((ext_vector_type(16)));
typedef float f4v __attribute__((ext_vector_type(4)));
typedef float f2v __attribute__((ext_vector_type(2)));
typedef short s8v __attribute__((ext_vector_type(8)));
typedef short s4v __attribute__((ext_vector_type(4)));
typedef __bf16 b2v __attribute__((ext_vector_type(2)));
#pragma clang diagnostic ignored "-Wint-to-pointer-cast"
#define LAS __attribute__((address_space(3)))
#define GAS __attribute__((address_space(1)))
#define DI __device__ __forceinline__
#define SB() __builtin_amdgcn_sched_barrier(0)
#define KILL() asm volatile("" : "=v"(acc))
#define NOUNROLL _Pragma("clang loop unroll(disable)")

constexpr u32 OFF_FLAG = 4096;
constexpr u32 OFF_FRG = 0x14000;
constexpr u32 OFF_DBG = 16384;
constexpr u32 OFF_STATS = 0x20000;
constexpr u32 OFF_Z2T = 0x60000;
constexpr u32 OFF_PKV = OFF_Z2T + 54 * 64 * 1024;
constexpr u32 OFF_PQ = OFF_PKV + 32 * 80 * 2 * 1024;
constexpr u32 OFF_Z1 = OFF_PQ + 32 * 40 * 4 * 512;
constexpr u32 OFF_YO = OFF_Z1 + 32 * 54 * 4 * 512;
constexpr u32 OFF_W3T = OFF_YO + 32 * 47 * 4 * 1024;
constexpr u32 OFF_KVP = OFF_W3T + 40 * 47 * 1024;
constexpr u32 OFF_KVF = OFF_KVP + 8 * 32 * 18 * 1024;
constexpr u32 OFF_FILL = OFF_KVF + 8 * 16384;
constexpr u32 WS_USED = OFF_FILL + 256 * 1024;
enum { E_Z2 = 0, E_KV = 1, E_Z1 = 3, E_Y = 4, E_Q = 5 };
constexpr u32 L_UP = 81920;
constexpr u32 L_PARK = L_UP;
constexpr u32 L_STAT = L_UP + 16384;
constexpr u32 L_NRM = L_UP + 24576;
constexpr u32 L_PART = L_UP + 25600;
constexpr u32 L_SCR = L_UP + 32768;
constexpr u32 L_FL = L_UP + 65536;
constexpr u32 L_PE = 151552;
constexpr u32 L_PL = 153600;
constexpr u32 L_JK = 163584;
constexpr u32 L_OC = 98304;
constexpr u32 L_JOB = 131072;
constexpr u32 L_WX = 102400;
constexpr u32 LDS_BYTES = 163840;
constexpr int SPIN = 1 << 15;
#define PRIO2 1
#define PF_SPA 2
#define PF_CODE2 1
#define PF_CODE 1
constexpr int PADB = 4, PADA = 0;

DI void glds2(const void* p, u32 v0, u32 v1, u32 lds) {
  asm volatile("s_mov_b32 m0, %3\n\ts_nop 0\n\tglobal_load_lds_dwordx4 %0, %2\n\tglobal_load_lds_dwordx4 %1, %2 offset:1024" ::"v"(v0), "v"(v1), "s"(p), "s"(lds) : "memory");
}
DI void glds4(const void* p, u32 v0, u32 v1, u32 v2, u32 v3, u32 lds) {
  asm volatile("s_mov_b32 m0, %5\n\ts_nop 0\n\tglobal_load_lds_dwordx4 %0, %4\n\tglobal_load_lds_dwordx4 %1, %4 offset:1024\n\t"
               "global_load_lds_dwordx4 %2, %4 offset:2048\n\tglobal_load_lds_dwordx4 %3, %4 offset:3072" ::"v"(v0), "v"(v1), "v"(v2), "v"(v3), "s"(p), "s"(lds) : "memory");
}
template <int N> DI void glds_run(const void* p, u32 v, u32 lds) {
  if (N == 1) asm volatile("s_mov_b32 m0, %2\n\ts_nop 0\n\tglobal_load_lds_dwordx4 %0, %1 sc1" ::"v"(v), "s"(p), "s"(lds) : "memory");
  if (N == 2) asm volatile("s_mov_b32 m0, %2\n\ts_nop 0\n\tglobal_load_lds_dwordx4 %0, %1 sc1\n\tglobal_load_lds_dwordx4 %0, %1 offset:1024 sc1" ::"v"(v), "s"(p), "s"(lds) : "memory");
  if (N == 3) asm volatile("s_mov_b32 m0, %2\n\ts_nop 0\n\tglobal_load_lds_dwordx4 %0, %1 sc1\n\tglobal_load_lds_dwordx4 %0, %1 offset:1024 sc1\n\t"
                           "global_load_lds_dwordx4 %0, %1 offset:2048 sc1" ::"v"(v), "s"(p), "s"(lds) : "memory");
  if (N == 4) asm volatile("s_mov_b32 m0, %2\n\ts_nop 0\n\tglobal_load_lds_dwordx4 %0, %1 sc1\n\tglobal_load_lds_dwordx4 %0, %1 offset:1024 sc1\n\t"
                           "global_load_lds_dwordx4 %0, %1 offset:2048 sc1\n\tglobal_load_lds_dwordx4 %0, %1 offset:3072 sc1" ::"v"(v), "s"(p), "s"(lds) : "memory");
}
template <int OFF> DI v4u gld16(const void* sbase, u32 voff) {
  v4u r;
  asm volatile("global_load_dwordx4 %0, %1, %2 offset:%3" : "=v"(r) : "v"(voff), "s"(sbase), "i"(OFF) : "memory");
  return r;
}
template <int OFF> DI v4u gld16_sc1(const void* sbase, u32 voff) {
  v4u r;
  asm volatile("global_load_dwordx4 %0, %1, %2 offset:%3 sc1" : "=v"(r) : "v"(voff), "s"(sbase), "i"(OFF) : "memory");
  return r;
}
template <int OFF> DI v2u gld8_sc1(const void* sbase, u32 voff) {
  v2u r;
  asm volatile("global_load_dwordx2 %0, %1, %2 offset:%3 sc1" : "=v"(r) : "v"(voff), "s"(sbase), "i"(OFF) : "memory");
  return r;
}
DI u32 gld4_sc1(const void* sbase, u32 voff) {
  u32 r;
  asm volatile("global_load_dword %0, %1, %2 sc1" : "=v"(r) : "v"(voff), "s"(sbase) : "memory");
  return r;
}
DI u32 gld2(const void* sbase, u32 voff) {
  u32 r;
  asm volatile("global_load_ushort %0, %1, %2" : "=v"(r) : "v"(voff), "s"(sbase) : "memory");
  return r;
}
DI void touch(const void* sbase, u32 voff, u32 junk) {
  asm volatile("s_mov_b32 m0, %2\n\ts_nop 4\n\tglobal_load_lds_dword %0, %1" ::"v"(voff), "s"(sbase), "s"(junk) : "memory");
}
DI void st16(v4u v, __amdgpu_buffer_rsrc_t rs, u32 vo, u32 so) {
  __builtin_amdgcn_raw_buffer_store_b128(v, rs, vo, so, 16);
  asm volatile("s_nop 1" ::"v"(v));
}
DI void st16x2(v4u a, v4u b, __amdgpu_buffer_rsrc_t rs, u32 vo, u32 d, u32 so) {
  __builtin_amdgcn_raw_buffer_store_b128(a, rs, vo, so, 16);
  __builtin_amdgcn_raw_buffer_store_b128(b, rs, vo, so + d, 16);
  asm volatile("s_nop 1" ::"v"(a), "v"(b));
}
#define WAIT_BAR(N) asm volatile("s_waitcnt vmcnt(%0) lgkmcnt(0)\n\ts_barrier" ::"i"(N) : "memory")
#define BAR() asm volatile("s_waitcnt lgkmcnt(0)\n\ts_barrier" ::: "memory")
#define BAR0() asm volatile("s_barrier" ::: "memory")
#define MARK()
#define KEEP()
#define DUMP(k, a, b)
#define SLOW(k)
#define DBG_ARG
#define DBG_PASS
#define STAMP(j)
#define CSTAMP(j)
template <class T> DI void tie(T& a) { asm volatile("" : "+v"(a)); }
template <int N> DI void waitv() { asm volatile("s_waitcnt vmcnt(%0)" ::"i"(N) : "memory"); }
template <int N, class T> DI void waitv(T& a) { asm volatile("s_waitcnt vmcnt(%1)" : "+v"(a) : "i"(N) : "memory"); }
template <int N, class T, class U> DI void waitv(T& a, U& b) { asm volatile("s_waitcnt vmcnt(%2)" : "+v"(a), "+v"(b) : "i"(N) : "memory"); }
DI v4u lds16(u32 a) { return *(const LAS v4u*)(a); }
DI f4v ldsf4(u32 a) { return *(const LAS f4v*)(a); }
DI void sts16(u32 a, v4u v) { *(LAS v4u*)(a) = v; }
DI v2u trd(u32 a) { return __builtin_bit_cast(v2u, __builtin_amdgcn_ds_read_tr16_b64_v4i16((LAS s4v*)(a))); }
DI v4u trd2(u32 a0, u32 a1) { v2u x = trd(a0), y = trd(a1); return v4u{x.x, x.y, y.x, y.y}; }
DI u32 pk2(f2v v) { return __builtin_bit_cast(u32, __builtin_convertvector(v, b2v)); }
DI u32 pk2(float a, float b) { return pk2(f2v{a, b}); }
DI float asf(u32 p) { return __builtin_bit_cast(float, p); }
DI u32 asu(float p) { return __builtin_bit_cast(u32, p); }
DI f2v up2(u32 p) { return f2v{asf(p << 16), asf(p & 0xffff0000u)}; }
DI f2v fma2(f2v a, f2v b, f2v c) { return __builtin_elementwise_fma(a, b, c); }
DI f2v lo2(f4v v) { return __builtin_shufflevector(v, v, 0, 1); }
DI f2v hi2(f4v v) { return __builtin_shufflevector(v, v, 2, 3); }
DI float dot2(u32 a, u32 b, float c) { return __builtin_amdgcn_fdot2_f32_bf16(__builtin_bit_cast(b2v, a), __builtin_bit_cast(b2v, b), c, false); }
DI f16v mfma32(v4u a, v4u b, f16v c) {
 return __builtin_amdgcn_mfma_f32_32x32x16_bf16(__builtin_bit_cast(s8v, a), __builtin_bit_cast(s8v, b), c, 0, 0, 0); }
DI f4v mfma16(v4u a, v4u b, f4v c) { return __builtin_amdgcn_mfma_f32_16x16x32_bf16(__builtin_bit_cast(s8v, a), __builtin_bit_cast(s8v, b), c, 0, 0, 0); }
DI f4v mfma16k(v2u a, v2u b, f4v c) { return __builtin_amdgcn_mfma_f32_16x16x16bf16_1k(__builtin_bit_cast(s4v, a), __builtin_bit_cast(s4v, b), c, 0, 0, 0); }
DI f16v zero16() { f16v z; for (int e = 0; e < 16; ++e) z[e] = 0.f; return z; }
template <int E> DI f2v pair(const f16v& a) { return __builtin_shufflevector(a, a, 2 * E, 2 * E + 1); }
DI float of(float v) { asm("" : "+v"(v)); return v; }
DI u32 pks(float a, float b) { return pk2(of(a), of(b)); }
DI float gelu1(float v) {
  const float a = v * __builtin_fmaf(v * v, -0.10294324f, -2.3022082f);
  return v * __builtin_amdgcn_rcpf(__builtin_amdgcn_exp2f(a) + 1.0f);
}
DI float phi1(float v) {
  return __builtin_amdgcn_fmed3f(__builtin_amdgcn_exp2f(v * 1.44269504f), 0.f, 1.f) + fmaxf(v + 1e-6f, 1e-6f);
}
DI void gelu4(float a, float b, float c, float d, u32& p0, u32& p1) {
  float t0, t1, t2, t3;
  asm("v_mul_f32 %2, %6, %6\n\tv_mul_f32 %3, %7, %7\n\tv_mul_f32 %4, %8, %8\n\tv_mul_f32 %5, %9, %9\n\t"
      "v_fmamk_f32 %2, %2, 0xbdd2d3e8, %10\n\tv_fmamk_f32 %3, %3, 0xbdd2d3e8, %10\n\tv_fmamk_f32 %4, %4, 0xbdd2d3e8, %10\n\tv_fmamk_f32 %5, %5, 0xbdd2d3e8, %10\n\t"
      "v_mul_f32 %2, %6, %2\n\tv_mul_f32 %3, %7, %3\n\tv_mul_f32 %4, %8, %4\n\tv_mul_f32 %5, %9, %5\n\t"
      "v_exp_f32 %2, %2\n\tv_exp_f32 %3, %3\n\tv_exp_f32 %4, %4\n\tv_exp_f32 %5, %5\n\t"
      "v_add_f32 %2, 1.0, %2\n\tv_add_f32 %3, 1.0, %3\n\tv_add_f32 %4, 1.0, %4\n\tv_add_f32 %5, 1.0, %5\n\t"
      "v_rcp_f32 %2, %2\n\tv_rcp_f32 %3, %3\n\tv_rcp_f32 %4, %4\n\tv_rcp_f32 %5, %5\n\t"
      "v_mul_f32 %2, %6, %2\n\tv_mul_f32 %3, %7, %3\n\tv_mul_f32 %4, %8, %4\n\tv_mul_f32 %5, %9, %5\n\t"
      "v_cvt_pk_bf16_f32 %0, %2, %3\n\tv_cvt_pk_bf16_f32 %1, %4, %5"
      : "=&v"(p0), "=&v"(p1), "=&v"(t0), "=&v"(t1), "=&v"(t2), "=&v"(t3) : "v"(a), "v"(b), "v"(c), "v"(d), "v"(-2.3022082f));
}
DI void gelu4s(float a, float b, float c, float d, u32& p0, u32& p1, float& s0, float& s1, float& q0, float& q1) {
  float t0, t1, t2, t3;
  asm("v_mul_f32 %2, %10, %10\n\tv_mul_f32 %3, %11, %11\n\tv_mul_f32 %4, %12, %12\n\tv_mul_f32 %5, %13, %13\n\t"
      "v_fmamk_f32 %2, %2, 0xbdd2d3e8, %14\n\tv_fmamk_f32 %3, %3, 0xbdd2d3e8, %14\n\tv_fmamk_f32 %4, %4, 0xbdd2d3e8, %14\n\tv_fmamk_f32 %5, %5, 0xbdd2d3e8, %14\n\t"
      "v_mul_f32 %2, %10, %2\n\tv_mul_f32 %3, %11, %3\n\tv_mul_f32 %4, %12, %4\n\tv_mul_f32 %5, %13, %5\n\t"
      "v_exp_f32 %2, %2\n\tv_exp_f32 %3, %3\n\tv_exp_f32 %4, %4\n\tv_exp_f32 %5, %5\n\t"
      "v_add_f32 %2, 1.0, %2\n\tv_add_f32 %3, 1.0, %3\n\tv_add_f32 %4, 1.0, %4\n\tv_add_f32 %5, 1.0, %5\n\t"
      "v_rcp_f32 %2, %2\n\tv_rcp_f32 %3, %3\n\tv_rcp_f32 %4, %4\n\tv_rcp_f32 %5, %5\n\t"
      "v_mul_f32 %2, %10, %2\n\tv_mul_f32 %3, %11, %3\n\tv_mul_f32 %4, %12, %4\n\tv_mul_f32 %5, %13, %5\n\t"
      "v_add_f32 %6, %6, %2\n\tv_add_f32 %7, %7, %3\n\tv_fmac_f32 %8, %2, %2\n\tv_fmac_f32 %9, %3, %3\n\t"
      "v_cvt_pk_bf16_f32 %0, %2, %3\n\tv_cvt_pk_bf16_f32 %1, %4, %5\n\t"
      "v_add_f32 %6, %6, %4\n\tv_add_f32 %7, %7, %5\n\tv_fmac_f32 %8, %4, %4\n\tv_fmac_f32 %9, %5, %5"
      : "=&v"(p0), "=&v"(p1), "=&v"(t0), "=&v"(t1), "=&v"(t2), "=&v"(t3), "+v"(s0), "+v"(s1), "+v"(q0), "+v"(q1)
      : "v"(a), "v"(b), "v"(c), "v"(d), "v"(-2.3022082f));
}
DI void phi4(float a, float b, float c, float d, u32& p0, u32& p1) {
  float t0, t1, t2, t3, u0, u1, u2, u3;
  asm("v_mul_f32 %2, 0x3fb8aa3b, %10\n\tv_mul_f32 %3, 0x3fb8aa3b, %11\n\tv_mul_f32 %4, 0x3fb8aa3b, %12\n\tv_mul_f32 %5, 0x3fb8aa3b, %13\n\t"
      "v_exp_f32_e64 %2, %2 clamp\n\tv_add_f32 %6, 0x358637bd, %10\n\tv_exp_f32_e64 %3, %3 clamp\n\tv_add_f32 %7, 0x358637bd, %11\n\t"
      "v_exp_f32_e64 %4, %4 clamp\n\tv_add_f32 %8, 0x358637bd, %12\n\tv_exp_f32_e64 %5, %5 clamp\n\tv_add_f32 %9, 0x358637bd, %13\n\t"
      "v_max_f32 %6, 0x358637bd, %6\n\tv_max_f32 %7, 0x358637bd, %7\n\tv_max_f32 %8, 0x358637bd, %8\n\tv_max_f32 %9, 0x358637bd, %9\n\t"
      "v_add_f32 %2, %2, %6\n\tv_add_f32 %3, %3, %7\n\tv_add_f32 %4, %4, %8\n\tv_add_f32 %5, %5, %9\n\t"
      "v_cvt_pk_bf16_f32 %0, %2, %3\n\tv_cvt_pk_bf16_f32 %1, %4, %5"
      : "=&v"(p0), "=&v"(p1), "=&v"(t0), "=&v"(t1), "=&v"(t2), "=&v"(t3), "=&v"(u0), "=&v"(u1), "=&v"(u2), "=&v"(u3)
      : "v"(a), "v"(b), "v"(c), "v"(d));
}
DI v4u norm8(v4u f, f4v c0, f4v c1, f4v h0, f4v h1) {
  v4u o;
  float t0, t1, t2, t3, t4, t5, t6, t7;
  asm("v_lshlrev_b32 %4, 16, %12\n\tv_and_b32 %5, 0xffff0000, %12\n\tv_lshlrev_b32 %6, 16, %13\n\tv_and_b32 %7, 0xffff0000, %13\n\t"
      "v_lshlrev_b32 %8, 16, %14\n\tv_and_b32 %9, 0xffff0000, %14\n\tv_lshlrev_b32 %10, 16, %15\n\tv_and_b32 %11, 0xffff0000, %15\n\t"
      "v_fma_f32 %4, %4, %16, %24\n\tv_fma_f32 %5, %5, %17, %25\n\tv_fma_f32 %6, %6, %18, %26\n\tv_fma_f32 %7, %7, %19, %27\n\t"
      "v_fma_f32 %8, %8, %20, %28\n\tv_fma_f32 %9, %9, %21, %29\n\tv_fma_f32 %10, %10, %22, %30\n\tv_fma_f32 %11, %11, %23, %31\n\t"
      "v_cvt_pk_bf16_f32 %0, %4, %5\n\tv_cvt_pk_bf16_f32 %1, %6, %7\n\tv_cvt_pk_bf16_f32 %2, %8, %9\n\tv_cvt_pk_bf16_f32 %3, %10, %11"
      : "=&v"(o.x), "=&v"(o.y), "=&v"(o.z), "=&v"(o.w), "=&v"(t0), "=&v"(t1), "=&v"(t2), "=&v"(t3), "=&v"(t4), "=&v"(t5), "=&v"(t6), "=&v"(t7)
      : "v"(f.x), "v"(f.y), "v"(f.z), "v"(f.w), "v"(c0.x), "v"(c0.y), "v"(c0.z), "v"(c0.w), "v"(c1.x), "v"(c1.y), "v"(c1.z), "v"(c1.w),
        "v"(h0.x), "v"(h0.y), "v"(h0.z), "v"(h0.w), "v"(h1.x), "v"(h1.y), "v"(h1.z), "v"(h1.w));
  return o;
}
DI u32 gelu2(f2v v) {
  const f2v a = v * fma2(v * v, f2v{-0.10294324f, -0.10294324f}, f2v{-2.3022082f, -2.3022082f});
  const f2v d = f2v{__builtin_amdgcn_exp2f(a.x), __builtin_amdgcn_exp2f(a.y)} + f2v{1.0f, 1.0f};
  return pk2(v * f2v{__builtin_amdgcn_rcpf(d.x), __builtin_amdgcn_rcpf(d.y)});
}
template <int CTRL> DI float dpp_add(float v) {
  return v + __builtin_bit_cast(float, __builtin_amdgcn_update_dpp(0, __builtin_bit_cast(int, v), CTRL, 0xf, 0xf, true));
}
DI float add32(float v) { auto r = __builtin_amdgcn_permlane32_swap(asu(v), asu(v), false, false); return asf(r[0]) + asf(r[1]); }
DI float add16(float v) { auto r = __builtin_amdgcn_permlane16_swap(asu(v), asu(v), false, false); return asf(r[0]) + asf(r[1]); }
DI int olane() {
  int l;
  asm volatile("v_mbcnt_lo_u32_b32 %0, -1, 0\n\tv_mbcnt_hi_u32_b32 %0, -1, %0" : "=v"(l));
  return l;
}
typedef const u8* cptr;
DI cptr karg(int i) {
  const __attribute__((address_space(4))) u8* ka = (const __attribute__((address_space(4))) u8*)__builtin_amdgcn_kernarg_segment_ptr();
  int o = 8 * i;
  asm volatile("" : "+s"(o));
  return *(const __attribute__((address_space(4))) cptr*)(ka + o);
}
DI u32 ov(u32 v) { asm volatile("" : "+v"(v)); return v; }
DI int osc(int v) { asm volatile("" : "+s"(v)); return v; }
template <class F, int... I> DI void sfor_(F&& f, std::integer_sequence<int, I...>) { (f(std::integral_constant<int, I>{}), ...); }
template <int N, class F> DI void sfor(F&& f) { sfor_(f, std::make_integer_sequence<int, N>{}); }
DI v4u tag_put(f4v c, u32 t8) { return v4u{(asu(c.x) & ~0xffu) | t8, asu(c.y), (asu(c.z) & ~0xffu) | t8, asu(c.w)}; }

#define RB 2
constexpr int cmin(int a, int b) { return a < b ? a : b; }
constexpr int at_mid(bool job, int i) { return (i + 4 < 8 ? 4 : 0) + (i == 5 || i == 6 ? 3 : 0) + (i == 0 ? 4 : 0) + (i == 3 ? 1 : 0) + (job ? (i == 4 ? 3 : 0) : (i == 3 ? 4 : 0)); }
constexpr int cnt_B(int NT, bool job, int j) {
  int n = NT * cmin(RB - 1, 15 - j);
  for (int m = (j - RB > 0 ? j - RB : 0); m < j; ++m) if (!(m & 1)) n += at_mid(job, m / 2);
  return cmin(n, 63);
}
constexpr int cnt_mid(int NT, bool job, int i0, int i1) {
  int n = 0;
  for (int j = 2 * i0 + 1; j <= 2 * i1; ++j) if (j + RB < 16) n += NT;
  for (int i = i0 + 1; i < i1; ++i) n += at_mid(job, i);
  return cmin(n, 63);
}
constexpr u32 slotA(int i) { return (u32)(3 - (i & 3)) * 32768u; }

DI void code_prefetch(const u8* start, u32 size, u32 junk) {
  const u32 o = (u32)(((blockIdx.x >> 3) & 7) * 64 + olane()) * 128;
  touch(start, min(o, size - 4), junk);
  touch(start, min(o + 65536, size - 4), junk);
}
struct Ctx {
  const u8* code;
  u32 csize;
  u8* ws;
  __amdgpu_buffer_rsrc_t wsr;
  u32 tag, t8;
  int w, rg, cb;
};
DI void post(const Ctx& c, int ev) {
  const u32 so = OFF_FRG + ev * 8192 + osc(c.rg) * 256 + c.cb * 4;
  if (ev == E_Z2 && olane() == 0) __builtin_amdgcn_raw_buffer_store_b32(c.tag, c.wsr, 0, OFF_FLAG + (c.rg * 8 + c.cb) * 4, 16);
  const u32 tg = osc(c.tag);
  if (olane() == 0) __builtin_amdgcn_raw_buffer_store_b32(tg, c.wsr, 0, so, 16);
}
DI bool all_eq(v4u f, u32 t) { u32 d = ((f.x ^ t) | (f.y ^ t)) | ((f.z ^ t) | (f.w ^ t)); asm volatile("" : "+v"(d)); return __builtin_amdgcn_ballot_w64(d != 0) == 0; }
DI void spin_all(const Ctx& c, int ev) {
  SLOW(8);
  NOUNROLL for (int s = 0; s < SPIN; ++s) {
    if (all_eq(__builtin_amdgcn_raw_buffer_load_b128(c.wsr, olane() * 16, OFF_FLAG + ev * 1024, 16), c.tag)) break;
    __builtin_amdgcn_s_sleep(1);
  }
}
DI void spin_rg(const Ctx& c, int ev) {
  SLOW(ev);
  NOUNROLL for (int s = 0; s < SPIN; ++s) {
    if (__builtin_amdgcn_ballot_w64(__builtin_amdgcn_raw_buffer_load_b32(c.wsr, (olane() & 7) * 4, OFF_FRG + ev * 8192 + c.rg * 256, 16) != c.tag) == 0) break;
    __builtin_amdgcn_s_sleep(1);
  }
}
DI u32 poll_rg(const Ctx& c, int ev) { return gld4_sc1(c.ws + OFF_FRG + ev * 8192 + c.rg * 256, (olane() & 7) * 4); }
DI void poll_lds(const Ctx& c, int ev, u32 dst) {
  asm volatile("s_mov_b32 m0, %2\n\ts_nop 0\n\tglobal_load_lds_dword %0, %1 sc1" ::"v"((olane() & 7) * 4), "s"(c.ws + OFF_FRG + ev * 8192 + c.rg * 256), "s"(dst) : "memory");
}
DI bool polled_lds(const Ctx& c, u32 src) { return __builtin_amdgcn_ballot_w64(*(const LAS u32*)(src + olane() * 4) != c.tag) == 0; }
DI void swap_tile(const u32 (&p)[8], v4u& o0, v4u& o1) {
  auto a = __builtin_amdgcn_permlane32_swap(p[0], p[2], false, false);
  auto c = __builtin_amdgcn_permlane32_swap(p[1], p[3], false, false);
  auto d = __builtin_amdgcn_permlane32_swap(p[4], p[6], false, false);
  auto e = __builtin_amdgcn_permlane32_swap(p[5], p[7], false, false);
  o0 = v4u{(u32)a[0], (u32)c[0], (u32)a[1], (u32)c[1]};
  o1 = v4u{(u32)d[0], (u32)e[0], (u32)d[1], (u32)e[1]};
}
DI v4u pair_tiles(v2u a, v2u b) {
  auto x = __builtin_amdgcn_permlane16_swap(a.x, b.x, false, false);
  auto y = __builtin_amdgcn_permlane16_swap(a.y, b.y, false, false);
  return v4u{(u32)x[0], (u32)y[0], (u32)x[1], (u32)y[1]};
}
DI u32 f_trb(int l) {
  const int q = (l & 15) >> 2, p = l & 3;
  return (8 * (l >> 4) + q) * 64 + (((p >> 1) ^ (q >> 1)) * 16) + (p & 1) * 8;
}
DI u32 f_vn(int l) { return ((l >> 4) & 1) * 512 + (l >> 5) * 256 + (l & 15) * 16; }

template <int NT, bool job  > DI void phase2(const Ctx& c, bool late, const f16v& acc1, u32 so_last, u64 epoch, v4u (&ya)[12][2] DBG_ARG) {
  u8* const ws = c.ws;
  const int w = c.w, rg = c.rg, cb = c.cb, q = w & 3, g = w >> 2;
  const int lane = olane();
  const int ft0 = (cb < 7 ? 7 * cb : 48) + (g ? (cb < 6 ? 4 : 3) : 0);
  const u8* pz = ws + OFF_Z2T + (size_t)ft0 * 1024;
  const int ch1 = q < 2 ? 9 + q : 2 + q, ch2 = q == 0 ? 15 : 5 + q;
  const u32 vl = lane * 16;
  v4u zb[RB][NT];
  auto loadZ1 = [&](int j, int n) {
    const int i = j >> 1, ch = i == 1 ? ch1 : (i == 2 ? ch2 : (i == 3 ? 11 + q : 4 * i + q));
    const u8* p = pz + (size_t)(2 * ch + (j & 1)) * 54 * 1024;
    if (n == 0) zb[j % RB][0] = gld16_sc1<0>(p, vl);
    if (n == 1) zb[j % RB][1] = gld16_sc1<1024>(p, vl);
    if (n == 2) zb[j % RB][2] = gld16_sc1<2048>(p, vl);
    if (n == 3) zb[j % RB][NT - 1] = gld16_sc1<3072>(p, vl);
  };
  auto loadZ = [&](int j) {
#pragma unroll
    for (int n = 0; n < NT; ++n) loadZ1(j, n);
  };
#pragma unroll
  for (int j = 0; j < RB; ++j) loadZ(j);
  SB();
  if (late) {
    u32 p[8]; v4u o0, o1;
    sfor<8>([&](auto e) { p[e.value] = gelu2(pair<e.value>(acc1)); });
    swap_tile(p, o0, o1);
    st16x2(o0, o1, c.wsr, f_vn(lane), 2048, so_last);
  }
  const int t16 = lane & 15, g4 = lane >> 4;
  u32 ra[4];
#pragma unroll
  for (int u = 0; u < 4; ++u) ra[u] = q * 8192 + (u ^ q) * 2048 + t16 * 128 + ((g4 ^ (t16 >> 1)) * 16);
  const u8* pA = karg(4) + (size_t)(64 * rg) * 4096 + (16 + (w >> 1)) * 128;
  const u32 va0 = (32 * (w & 1) + (lane >> 3)) * 4096 + (((lane & 7) ^ g4) * 16), va1 = (va0 ^ 64) + 31744;
  const u32 va2 = va0 + 2 * 31744, va3 = va1 + 2 * 31744;
  STAMP(6); MARK();
  if (blockIdx.x == 0 && w == 2 && lane == 0) __hip_atomic_store((u64*)ws, epoch, __ATOMIC_RELAXED, __HIP_MEMORY_SCOPE_AGENT);
  STAMP(7);
  const int kpr = rg < 13 ? rg : (rg < 26 ? rg - 13 : 0);
  const int kti = kpr < 5 ? kpr : (kpr < 9 ? kpr - 5 : kpr - 8), ktj = kpr < 5 ? kpr : (kpr < 9 ? kpr - 4 : kpr - 9);
  const u32 kso = OFF_KVP + (cb * 18 + (rg < 13 ? rg : 13 + kti)) * 32768;
  auto fetchQ = [&](v2u (&f)[4]) {
    const u8* pp = ws + OFF_PQ + (size_t)((rg * 40 + 5 * cb + (job ? 2 : 0)) * 4 + q) * 512;
    const u32 vo = (g4 >> 1) * 256 + t16 * 16 + (g4 & 1) * 8;
    f[0] = gld8_sc1<0>(pp, vo); f[1] = gld8_sc1<2048>(pp, vo); f[2] = gld8_sc1<0>(pp + 4096, vo);
    if (!job) f[3] = gld8_sc1<2048>(pp + 4096, vo);
  };
  auto fetchW = [&](int qq) {
    const u8* p = ws + OFF_W3T + (size_t)(cb * 47 + 5 * qq) * 5120 + q * 4096;
    if (q < 3) glds_run<4>(p, vl, L_WX + qq * 15360 + q * 4096); else glds_run<3>(p, vl, L_WX + qq * 15360 + q * 4096);
  };
  f4v acc[NT][4];
  v4u af[2][4];
  u32 bsv = 0, fl = 0;
  v2u pqf[4] = {{0, 0}, {0, 0}, {0, 0}, {0, 0}}, z1v[NT];
#pragma unroll
  for (int u = 0; u < 4; ++u) af[0][u] = lds16(slotA(0) + ra[u]);
  if (job) __builtin_amdgcn_s_setprio(PRIO2);
  sfor<16>([&](auto jc) {
    constexpr int j = jc.value, i = j >> 1;
    if (j < 15) {
#pragma unroll
      for (int u = 0; u < 4; ++u) af[(j + 1) & 1][u] = (j & 1) ? lds16(slotA(i + 1) + ra[u]) : lds16(slotA(i) + (ra[u] ^ 64));
    }
    if (NT == 4) asm volatile("s_waitcnt vmcnt(%4)" : "+v"(zb[j % RB][0]), "+v"(zb[j % RB][1]), "+v"(zb[j % RB][2]), "+v"(zb[j % RB][NT - 1]) : "i"(cnt_B(NT, job, j)) : "memory");
    else asm volatile("s_waitcnt vmcnt(%3)" : "+v"(zb[j % RB][0]), "+v"(zb[j % RB][1]), "+v"(zb[j % RB][2]) : "i"(cnt_B(NT, job, j)) : "memory");
    SB();
#pragma unroll
    for (int n = 0; n < NT; ++n) {
#pragma unroll
      for (int u = 0; u < 4; ++u)
        acc[n][u] = mfma16(zb[j % RB][n], af[j & 1][u], j == 0 ? f4v{0.f, 0.f, 0.f, 0.f} : acc[n][u]);
      SB();
      if (j + RB < 16) loadZ1(j + RB, n);
      SB();
    }
    if (j == 13) {
      const u8* p = ws + OFF_Z1 + (size_t)((rg * 54 + ft0) * 4 + q) * 512;
      const u32 vo = (g4 >> 1) * 256 + t16 * 16 + (g4 & 1) * 8;
      z1v[0] = gld8_sc1<0>(p, vo); z1v[1] = gld8_sc1<2048>(p, vo); z1v[2] = gld8_sc1<0>(p + 4096, vo);
      if (NT == 4) z1v[NT - 1] = gld8_sc1<2048>(p + 4096, vo);
    }
    if (!(j & 1) && i < 7) {
      if (i == 1 || i == 2 || i == 5 || i == 6) MARK();
      if (i == 2 && job && rg < 26) {
        f4v s;
        u32 bad;
        const u32 vo = g4 * 1024 + (16 * q + (rg < 13 ? t16 : 0)) * 16, ja = ov(L_JOB + vo);
        auto take = [&](v4u v, bool first) {
          const u32 t = (v.x ^ c.t8) | (v.z ^ c.t8);
          const float x = asf(v.x & ~0xffu), z = asf(v.z & ~0xffu);
          bad = first ? t : bad | t;
          s = first ? f4v{x, asf(v.y), z, asf(v.w)} : f4v{of(s.x + x), of(s.y + asf(v.y)), of(s.z + z), of(s.w + asf(v.w))};
        };
        v4u v[8];
#pragma unroll
        for (int k = 0; k < 8; ++k) v[k] = lds16(ja + k * 4096);
        const bool live = ((16 * ktj + t16) * 205) >> 12 == ((16 * kti + 4 * q) * 205) >> 12;
        SB();
#pragma unroll
        for (int k = 0; k < 8; ++k) take(v[k], k == 0);
        if (__builtin_amdgcn_ballot_w64((bad & 0xff) != 0 && live) != 0) {
          SLOW(9);
          NOUNROLL for (int sp = 0; sp < SPIN; ++sp) {
#pragma unroll
            for (int k = 0; k < 8; ++k) take(__builtin_amdgcn_raw_buffer_load_b128(c.wsr, vo + k * 4096, kso, 16), k == 0);
            if (__builtin_amdgcn_ballot_w64((bad & 0xff) != 0 && live) == 0) break;
            __builtin_amdgcn_s_sleep(1);
          }
        }
        auto red = [&](float x) -> float { return __builtin_amdgcn_mfma_f32_16x16x4f32(1.0f, live ? x : 0.f, f4v{0.f, 0.f, 0.f, 0.f}, 0, 0, 0)[0]; };
        const float r0 = red(s.x), r1 = red(s.y), r2 = red(s.z), r3 = red(s.w);
        if (lane < 16) __builtin_amdgcn_raw_buffer_store_b64(v2u{pks(r0, r1), pks(r2, r3)}, c.wsr, lane * 8, OFF_KVF + cb * 16384 + rg * 512 + q * 128, 16);
      }
      if (i == 6) {
        asm volatile("s_waitcnt vmcnt(%4)" : "+v"(pqf[0]), "+v"(pqf[1]), "+v"(pqf[2]), "+v"(pqf[3]) : "i"(cnt_mid(NT, job, 4, 6)) : "memory");
        MARK();
        const u32 a = ov(L_JOB + vl / 2);
        bsv = *(const LAS unsigned short*)(L_OC + (16 * q + t16) * 2);
        constexpr int J0 = job ? 3 : 0, NJ = job ? 2 : 3, P0 = job ? 2 : 0;
        v2u tn[NJ][3], td[NJ][3];
        u32 m;
        auto gather = [&](auto&& tl) {
          m = 0;
          auto one = [&](int x) -> v2u { const v2u t = tl(x); m = max(m, max(t.x, t.y)); return t; };
#pragma unroll
          for (int k = 0; k < NJ; ++k) {
            const int tj = J0 + k;
            tn[k][0] = one(tj); td[k][0] = one(13 + tj);
            tn[k][1] = tj > 0 ? one(4 + tj) : v2u{0, 0}; td[k][1] = tj > 0 ? one(17 + tj) : v2u{0, 0};
            tn[k][2] = tj < 4 ? one(9 + tj) : v2u{0, 0}; td[k][2] = tj < 4 ? one(22 + tj) : v2u{0, 0};
          }
        };
        gather([&](int x) -> v2u { return *(const LAS v2u*)(a + x * 512); });
        if (__builtin_amdgcn_ballot_w64(m == ~0u) != 0) {
          SLOW(10);
          NOUNROLL for (int sp = 0; sp < SPIN; ++sp) {
            gather([&](int x) -> v2u { return __builtin_amdgcn_raw_buffer_load_b64(c.wsr, vl / 2, OFF_KVF + cb * 16384 + x * 512, 16); });
            if (__builtin_amdgcn_ballot_w64(m == ~0u) == 0) break;
            __builtin_amdgcn_s_sleep(1);
          }
        }
        f4v num[NJ], den[NJ];
#pragma unroll
        for (int k = 0; k < NJ; ++k) {
          num[k] = mfma16k(tn[k][0], pqf[J0 + k - P0], f4v{0.f, 0.f, 0.f, 0.f});
          den[k] = mfma16k(td[k][0], pqf[J0 + k - P0], f4v{1e-6f, 1e-6f, 1e-6f, 1e-6f});
        }
#pragma unroll
        for (int k = 0; k < NJ; ++k)
          if (J0 + k > 0) { num[k] = mfma16k(tn[k][1], pqf[J0 + k - 1 - P0], num[k]); den[k] = mfma16k(td[k][1], pqf[J0 + k - 1 - P0], den[k]); }
#pragma unroll
        for (int k = 0; k < NJ; ++k)
          if (J0 + k < 4) { num[k] = mfma16k(tn[k][2], pqf[J0 + k + 1 - P0], num[k]); den[k] = mfma16k(td[k][2], pqf[J0 + k + 1 - P0], den[k]); }
        const int ct = 5 * cb + J0;
        const u32 so = OFF_YO + rg * 47 * 4096 + q * 1024 + ct * 2048 - (ct & 1) * 1536;
        v2u o[NJ];
#pragma unroll
        for (int k = 0; k < NJ; ++k)
          o[k] = v2u{pks(num[k].x * __builtin_amdgcn_rcpf(den[k].x), num[k].y * __builtin_amdgcn_rcpf(den[k].y)),
                     pks(num[k].z * __builtin_amdgcn_rcpf(den[k].z), num[k].w * __builtin_amdgcn_rcpf(den[k].w))};
        st16(pair_tiles(o[0], o[1]), c.wsr, (g4 >> 1) * 256 + t16 * 16 + (g4 & 1) * ((ct & 1) ? 3584 : 512), so);
        if (!job) __builtin_amdgcn_raw_buffer_store_b64(o[NJ - 1], c.wsr, (g4 >> 1) * 256 + t16 * 16 + (g4 & 1) * 8, so + 4096, 16);
      }
      if (i == 1) waitv<cnt_mid(NT, job, 0, 1)>();
      if (i == 3 && job && w == 4) {
        waitv<cnt_mid(NT, job, 1, 3)>(fl);
        if (__builtin_amdgcn_ballot_w64(fl != c.tag) != 0) spin_rg(c, E_Q);
      }
      if (i == 4) waitv<cnt_mid(NT, job, 1, 4)>();
      if (i == 6 && !job && w == 2) {
        tie(fl);
        if (__builtin_amdgcn_ballot_w64(fl != c.tag) != 0) spin_rg(c, E_Z1);
      }
      if (i == 5) { if (job && w == 7) waitv<cnt_mid(NT, job, 4, 5)>(); else waitv<cnt_mid(NT, job, 3, 5)>(); }
      if (i == 1 || i == 2 || i == 5 || i == 6) MARK();
      BAR();
      if (i + 4 < 8) { glds4(pA, va0, va1, va2, va3, slotA(i) + w * 4096); pA += 512; }
      if (i == 0) glds_run<4>(ws + kso + w * 4096, vl, L_JOB + w * 4096);
      if (i == 2 && !job) touch(c.code, min((u32)((((blockIdx.x >> 3) * 4 + q) * 4 + (lane & 3))) * 128, c.csize - 4), L_JK);
      if (i == 0 && w == 1) post(c, E_Q);
      if (i == 2 && w == 1) post(c, E_Z1);
      if (i == 1 && job && w == 4) fl = poll_rg(c, E_Q);
      if (i == 3) {
        if (w < 5) glds_run<2>(ws + OFF_KVF + cb * 16384 + w * 2048, vl, L_JOB + w * 2048);
        else glds_run<1>(ws + OFF_KVF + cb * 16384 + (5 + w) * 1024, vl, L_JOB + (5 + w) * 1024);
        if (!job) fetchQ(pqf);
      }
      if (i == 4) {
        if (job) {
          fetchQ(pqf);
          if (w == 7) touch(karg(5) + 128 * rg, min(lane, 31) * 4, L_OC);
        } else if (w == 2) fl = poll_rg(c, E_Z1);
      }
      if (i == 5) fetchW(3 * g);
      if (i == 6) fetchW(1 + g);
    }
  });
  __builtin_amdgcn_s_setprio(0);
  STAMP(8); MARK();
  if (NT == 4) asm volatile("s_waitcnt vmcnt(0)" : "+v"(z1v[0]), "+v"(z1v[1]), "+v"(z1v[2]), "+v"(z1v[NT - 1]) :: "memory");
  else asm volatile("s_waitcnt vmcnt(0)" : "+v"(z1v[0]), "+v"(z1v[1]), "+v"(z1v[2]) :: "memory");
  BAR();
  const u32 rb = (g ? (cb < 6 ? 4 : 3) : 0) * 12288 + vl;
  const u8* pyo = ws + OFF_YO + (size_t)(rg * 47 + 5 * q) * 4096 + g * 2048;
  auto loadO = [&](int s) { ya[s][0] = gld16_sc1<0>(pyo + s * 4096, vl); ya[s][1] = gld16_sc1<1024>(pyo + s * 4096, vl); };
#pragma unroll
  for (int u = 1; u < 4; ++u) {
    const u32 a = ov(rb + ((u ^ q) * 3 + u - 1) * 1024);
#pragma unroll
    for (int n = 0; n < NT; ++n) *(LAS f4v*)(a + n * 12288) = acc[n][u];
    SB();
    loadO(u - 1);
    SB();
  }
  BAR();
  loadO(3); loadO(4);
  {
    const float bb = asf(bsv << 16);
    const f4v b4 = {bb, bb, bb, bb};
    const u32 ra0 = ov(rb + q * 3072);
    const u32 so = OFF_YO + (rg * 47 + 20) * 4096 + q * 1024 + ft0 * 2048 - (ft0 & 1) * 1536;
    v2u y[NT];
#pragma unroll
    for (int n = 0; n < NT; ++n) {
      const u32 a = ra0 + n * 12288;
      const f4v s = ((acc[n][0] + b4) + ldsf4(a)) + (ldsf4(a + 1024) + ldsf4(a + 2048));
      y[n] = v2u{pk2(up2(z1v[n].x) * lo2(s)), pk2(up2(z1v[n].y) * hi2(s))};
    }
    const u32 vp = (g4 >> 1) * 256 + t16 * 16 + (g4 & 1) * ((ft0 & 1) ? 3584 : 512);
    st16(pair_tiles(y[0], y[1]), c.wsr, vp, so);
    if (NT == 4) st16(pair_tiles(y[2], y[NT - 1]), c.wsr, vp, so + 4096);
    else __builtin_amdgcn_raw_buffer_store_b64(y[2], c.wsr, (g4 >> 1) * 256 + t16 * 16 + (g4 & 1) * 8, so + 4096, 16);
  }
}

DI void phase3(const Ctx& c, v4u (&ya)[12][2] DBG_ARG) {
  u8* const ws = c.ws;
  const int w = c.w, rg = c.rg, cb = c.cb, q = w & 3, g = w >> 2;
  const int lane = olane();
  const u32 vl = lane * 16;
  const u8* pwo = ws + OFF_W3T + (size_t)(cb * 47 + 5 * q) * 5120;
  const u8* pwy = ws + OFF_W3T + (size_t)(cb * 47 + 20 + 7 * q) * 5120;
  const u8* pyy = ws + OFF_YO + (size_t)(rg * 47 + 20 + 7 * q) * 4096 + g * 2048;
  auto loadY = [&](int s) {
    const u8* p = pyy + (size_t)(s == 11 && q == 3 ? 0 : s - 5) * 4096;
    ya[s][0] = gld16_sc1<0>(p, vl); ya[s][1] = gld16_sc1<1024>(p, vl);
  };
  v4u xv0, xv1;
  f4v oc[5][2];
  v4u wr[2][5];
  const u32 rx = ov(L_WX + q * 15360 + vl), ry = ov(q * 25600 + vl);
  auto wrow = [&](int s) -> u32 { return s < 3 ? rx + s * 5120 : (s < 8 ? ry + (s - 3) * 5120 : (s < 11 ? rx + (s - 8) * 5120 : ry)); };
  auto rd = [&](int s) {
#pragma unroll
    for (int n = 0; n < 5; ++n) wr[s & 1][n] = lds16(wrow(s) + n * 1024);
  };
  auto step = [&](int s) {
    SB();
#pragma unroll
    for (int n = 0; n < 5; ++n)
#pragma unroll
      for (int tt = 0; tt < 2; ++tt) oc[n][tt] = mfma16(wr[s & 1][n], ya[s][tt], s == 0 ? f4v{0.f, 0.f, 0.f, 0.f} : oc[n][tt]);
    SB();
  };
  auto fresh = [&](int s) {
    const v4u a = ya[s][0], b = ya[s][1];
    u32 m = max(max(max(a.x, a.y), a.z), max(max(a.w, b.x), max(max(b.y, b.z), b.w)));
    if (__builtin_amdgcn_ballot_w64(m == ~0u) != 0) {
      SLOW(s < 5 ? 2 : 6);
      const u32 so = OFF_YO + (rg * 47 + (s < 5 ? 5 * q + s : 15 + 7 * q + s)) * 4096 + g * 2048;
      v4u c0, c1;
      NOUNROLL for (int sp = 0; sp < 4096; ++sp) {
        if (sp == 4095) spin_rg(c, E_Y);
        c0 = __builtin_amdgcn_raw_buffer_load_b128(c.wsr, vl, so, 16);
        c1 = __builtin_amdgcn_raw_buffer_load_b128(c.wsr, vl, so + 1024, 16);
        m = max(max(max(c0.x, c0.y), c0.z), max(max(c0.w, c1.x), max(max(c1.y, c1.z), c1.w)));
        if (__builtin_amdgcn_ballot_w64(m == ~0u) == 0) break;
        __builtin_amdgcn_s_sleep(1);
      }
      ya[s][0] = c0; ya[s][1] = c1;
    }
  };
  rd(0); rd(1);
  BAR();
  const u32 dq = q * 25600, dx = L_WX + q * 15360;
  glds_run<4>(pwo + 15360 + g * 5120, vl, dq + g * 5120); glds_run<1>(pwo + 19456 + g * 5120, vl, dq + 4096 + g * 5120);
  waitv<5 + 2 + 8>(ya[0][0], ya[0][1]);
  MARK();
  fresh(0); step(0);
  glds_run<4>(pwy + g * 8192, vl, dq + 10240 + g * 8192);
  rd(2);
  waitv<9 + 2 + 6>(ya[1][0], ya[1][1]);
  fresh(1); step(1);
  if (g == 0) glds_run<4>(pwy + 4096, vl, dq + 14336); else glds_run<3>(pwy + 12288, vl, dq + 22528);
  asm volatile("s_waitcnt vmcnt(7)" : "+v"(ya[2][0]), "+v"(ya[2][1]), "+v"(ya[3][0]), "+v"(ya[3][1]), "+v"(ya[4][0]), "+v"(ya[4][1]) :: "memory");
  BAR();
  if (w == 1) post(c, E_Y);
  STAMP(9);
  rd(3);
  loadY(5); loadY(6);
  fresh(2); step(2); fresh(3);
  rd(4);
  loadY(7); loadY(8);
  step(3); fresh(4);
  glds_run<4>(pwy + 15360 + g * 8192, vl, dx + g * 8192);
  WAIT_BAR(12);
  STAMP(10);
  rd(5);
  step(4);
  if (g == 0) glds_run<4>(pwy + 19456, vl, dx + 4096); else glds_run<3>(pwy + 27648, vl, dx + 12288);
  rd(6);
  waitv<6 + 7>(ya[5][0], ya[5][1]);
  MARK();
  fresh(5); step(5);
  waitv<4 + 7>(ya[6][0], ya[6][1]);
  fresh(6);
  loadY(9); loadY(10);
  rd(7);
  step(6);
  loadY(11);
  const int tid = w * 64 + lane;
  const int t0 = (tid * 6554) >> 16, c0 = tid - t0 * 10, u1 = w < 2 ? tid + 512 : tid, t1 = (u1 * 6554) >> 16, c1 = u1 - t1 * 10;
  const u32 go0 = t0 * 1280 + c0 * 16, go1 = t1 * 1280 + c1 * 16;
  const u8* px = karg(0) + (size_t)(64 * rg) * 1280 + cb * 160;
  const u32 a0 = ov(((c0 >> 1) * 4 + (t0 >> 4)) * 1024 + (c0 & 1) * 512 + ((t0 + 3 * c0) & 15) * 16);
  const u32 a1 = ov(((c1 >> 1) * 4 + (t1 >> 4)) * 1024 + (c1 & 1) * 512 + ((t1 + 3 * c1) & 15) * 16);
  u8* const po = (u8*)karg(7) + (size_t)(64 * rg) * 1280 + cb * 160;
  asm volatile("s_waitcnt vmcnt(6)" : "+v"(ya[7][0]), "+v"(ya[7][1]), "+v"(ya[8][0]), "+v"(ya[8][1]) :: "memory");
  fresh(7); fresh(8);
  BAR();
  if (q < 3) {
    if (g == 0) glds_run<2>(pwy + 30720, vl, dq); else glds_run<3>(pwy + 32768, vl, dq + 2048);
  }
  xv0 = gld16<0>(px, go0); xv1 = gld16<0>(px, go1);
  rd(8);
  step(7);
  rd(9);
  step(8);
  asm volatile("s_waitcnt vmcnt(4)" : "+v"(ya[9][0]), "+v"(ya[9][1]), "+v"(ya[10][0]), "+v"(ya[10][1]) :: "memory");
  fresh(9); fresh(10);
  rd(10);
  step(9);
  asm volatile("s_waitcnt vmcnt(0)" : "+v"(ya[11][0]), "+v"(ya[11][1]), "+v"(xv0), "+v"(xv1) :: "memory");
  if (q < 3) fresh(11);
  BAR();
  rd(11);
  step(10);
  if (q < 3) step(11);
  u32 rr[5];
#pragma unroll
  for (int n = 0; n < 5; ++n) rr[n] = ov(q * 20480 + g * 2048 + n * 4096 + (lane >> 4) * 256 + ((lane + 6 * n + 3 * (lane >> 5)) & 15) * 16);
  STAMP(11);
  BAR();
#pragma unroll
  for (int n = 0; n < 5; ++n)
#pragma unroll
    for (int tt = 0; tt < 2; ++tt) *(LAS f4v*)(rr[n] + tt * 1024) = oc[n][tt];
  BAR();
  auto finish = [&](u32 a, u32 go, v4u xv) {
    const f4v lo = (ldsf4(a) + ldsf4(a + 20480)) + (ldsf4(a + 40960) + ldsf4(a + 61440));
    const f4v hi = (ldsf4(a + 256) + ldsf4(a + 20480 + 256)) + (ldsf4(a + 40960 + 256) + ldsf4(a + 61440 + 256));
    const v4u o = {pk2(up2(xv.x) + lo2(lo)), pk2(up2(xv.y) + hi2(lo)), pk2(up2(xv.z) + lo2(hi)), pk2(up2(xv.w) + hi2(hi))};
    *(GAS v4u*)(po + go) = o;
  };
  finish(a0, go0, xv0);
  if (w < 2) finish(a1, go1, xv1);
  STAMP(12); CSTAMP(31); MARK(); MARK();
}

__global__ __launch_bounds__(512, 2) void fused(const u8* __restrict__ x, const u8* __restrict__ wqkv, const u8* __restrict__ wo,
                                                const u8* __restrict__ win, const u8* __restrict__ wsp, const u8* __restrict__ bsp,
                                                const u8* __restrict__ wout, u8* __restrict__ out, u8* ws) {
  extern __shared__ __attribute__((aligned(16))) u8 smem[];
  const int lane = threadIdx.x & 63;
  const int w = __builtin_amdgcn_readfirstlane(threadIdx.x >> 6);
  const int b = blockIdx.x, xg = b & 7, jb = b >> 3;
  const int rg = 8 * (xg >> 1) + (jb & 7), cb = 4 * (xg & 1) + (jb >> 3);
  const int g = w >> 2, wq4 = w & 3, mh = w & 1, hf = (w >> 1) & 1;
  Ctx c;
  c.ws = ws; c.w = w; c.rg = rg; c.cb = cb;
  c.wsr = __builtin_amdgcn_make_buffer_rsrc(ws, 0, WS_USED, 0x00020000);
  const auto wsr = c.wsr;
  asm volatile("s_getpc_b64 %0\n.Lk_begin:\n\ts_mov_b32 %1, .Lk_end-.Lk_begin" : "=s"(c.code), "=s"(c.csize));
  MARK();
  STAMP(0); CSTAMP(30);
  const int r8 = lane >> 3, s8 = lane & 7;
  v4u xr[10];
  {
    const u8* xb = x + (size_t)(64 * rg + 8 * w) * 1280;
    const u32 vo = r8 * 1280 + s8 * 16;
    xr[0] = gld16<0>(xb, vo); xr[1] = gld16<128>(xb, vo); xr[2] = gld16<256>(xb, vo); xr[3] = gld16<384>(xb, vo); xr[4] = gld16<512>(xb, vo);
    xr[5] = gld16<640>(xb, vo); xr[6] = gld16<768>(xb, vo); xr[7] = gld16<896>(xb, vo); xr[8] = gld16<1024>(xb, vo); xr[9] = gld16<1152>(xb, vo);
  }
  v2u ep = gld8_sc1<0>(ws + (w & 0) , 0u);
  asm volatile("s_barrier" ::: "memory");
  const u32 blksA = (u32)(43 + cb) | (u32)(12 + cb) << 8 | (u32)(cb + (cb < 2 ? 28 : -2)) << 16 | (u32)(34 + cb) << 24;
  const u32 blksB = (u32)(cb + (cb < 6 ? 51 : 4)) | (u32)(20 + cb) << 8 | (u32)(cb + (cb < 4 ? 6 : 26)) << 16 | 42u << 24;
  const u32 blks = g ? blksB : blksA;
  const int nr = (g == 0 || cb == 7) ? 4 : 3;
  const bool late = (g == 0) != (cb == 7);
  const u8* const pAu = wsp + (size_t)(64 * rg) * 4096 + (g ? 6 : 11) * 128;
  const u8* const winb = win - 30 * 128;
  const u8 *pw, *pwn;
  u32 stw, stwn, vo0, vo1, vo0n, vo1n;
  auto next_desc = [&](int r) {
    const int l = olane();
    const bool isA = r >= nr;
    const u32 bk = ((u32)osc(blks) >> ((8 * r) & 31)) & 0xff;
    const bool isq = bk < 30;
    const u8* pb = isq ? wqkv : winb;
    pwn = isA ? pAu : pb + bk * 128;
    const u32 d = isA ? 4096u : (isq ? 3840u : 3456u);
    stwn = isA ? 128u : 64 * d;
    const u32 xx = (u32)(l >> 4), m = (xx & (isA ? 3u : 0u)) | ((xx << 2) & (isA ? 0u : 4u));
    vo0n = (16 * wq4 + (l >> 3)) * d + (((l & 7) ^ m) * 16);
    vo1n = (vo0n ^ (isA ? 64u : 0u)) + 8 * d - 1024;
  };
#define TW 32
  next_desc(0);
  pw = pwn; stw = stwn; vo0 = vo0n; vo1 = vo1n;
  next_desc(1);
  const u32 ring = g * 40960;
  const u32 wl = ring + wq4 * 2048;
  asm volatile("s_waitcnt vmcnt(0)"
               : "+v"(xr[0]), "+v"(xr[1]), "+v"(xr[2]), "+v"(xr[3]), "+v"(xr[4]), "+v"(xr[5]), "+v"(xr[6]), "+v"(xr[7]), "+v"(xr[8]), "+v"(xr[9]), "+v"(ep)
               :: "memory");
  auto prefill = [&](int i0, int i1) {
#pragma unroll
    for (int i = i0; i < i1; ++i) { glds2(pw, vo0, vo1, wl + i * 8192); pw += stw; }
  };
  if (g == 0) {
    prefill(0, 4);
    const u32 s4 = b * 4 + wq4, jk = wl + 4 * 8192;
    touch(wq4 < 2 ? win : wqkv, min((b * 2 + (wq4 & 1)) * 38 + min(lane, 37), wq4 < 2 ? 17279 : 19199) * 128, jk);
    touch(s4 < 427 ? wo : wout, min((s4 < 427 ? s4 : s4 - 427) * 15 + min(lane, 14), s4 < 427 ? 6399u : 8639u) * 128, jk);
    if (PF_CODE) touch(c.code, min((u32)((jb * 4 + wq4) * 4 + (lane & 3)) * 128, c.csize - 4), jk);
    prefill(4, 5);
    asm volatile("" ::: "memory");
  }
  STAMP(1); MARK();
  const u64 epoch = (((u64)(u32)__builtin_amdgcn_readfirstlane(ep.y) << 32) | (u32)__builtin_amdgcn_readfirstlane(ep.x)) + 1;
  c.t8 = (u32)epoch & 0xffu;
  c.tag = (u32)epoch * 0x9E3779B1u ^ (u32)(epoch >> 32);
  {
    float a1[4] = {0.f, 0.f, 0.f, 0.f}, a2[4] = {0.f, 0.f, 0.f, 0.f};
#pragma unroll
    for (int j = 0; j < 10; ++j) {
#pragma unroll
      for (int e = 0; e < 4; ++e) a1[e] = dot2(xr[j][e], 0x3f803f80u, a1[e]);
#pragma unroll
      for (int e = 0; e < 4; ++e) a2[e] = dot2(xr[j][e], xr[j][e], a2[e]);
    }
    float s1 = (a1[0] + a1[1]) + (a1[2] + a1[3]), s2 = (a2[0] + a2[1]) + (a2[2] + a2[3]);
    s1 = dpp_add<0xB1>(s1); s2 = dpp_add<0xB1>(s2); s1 = dpp_add<0x4E>(s1); s2 = dpp_add<0x4E>(s2); s1 = dpp_add<0x141>(s1); s2 = dpp_add<0x141>(s2);
    const float mean = s1 * (1.0f / 640.0f);
    const float rs = __builtin_amdgcn_rsqf(fmaxf(s2 * (1.0f / 640.0f) - mean * mean, 0.f) + 1e-5f), sh = -mean * rs;
    const f2v rs2 = {rs, rs}, sh2 = {sh, sh};
    const int row = 8 * w + r8;
    const u32 rb0 = L_UP + row * 1280 + ((s8 ^ (row & 15)) * 16), rb1 = rb0 ^ 128;
#pragma unroll
    for (int j = 0; j < 10; j += 2) {
      f2v u[8];
#pragma unroll
      for (int e = 0; e < 8; ++e) u[e] = up2(xr[j + (e >> 2)][e & 3]);
#pragma unroll
      for (int e = 0; e < 8; ++e) u[e] = fma2(u[e], rs2, sh2);
      sts16(rb0 + (j >> 1) * 256, v4u{pk2(u[0]), pk2(u[1]), pk2(u[2]), pk2(u[3])});
      sts16(rb1 + (j >> 1) * 256, v4u{pk2(u[4]), pk2(u[5]), pk2(u[6]), pk2(u[7])});
    }
  }
  if (g == 0) waitv<8>();
  BAR();
  STAMP(2);
  v4u xa[40], wf[2][4];
  const u32 tb = ov(ring + (8 * (lane >> 5) + ((lane & 15) >> 2)) * 128 + ((hf ^ ((lane & 15) >> 3)) * 64) + ((lane >> 4) & 1) * 32 + (lane & 3) * 8);
  u32 xb[8];
  {
    const int row = 32 * mh + (lane & 31);
    const u32 rb = L_UP + row * 1280, X = (u32)((lane >> 5) ^ (row & 15));
#pragma unroll
    for (int k = 0; k < 8; ++k) xb[k] = ov(rb + (((2 * k) ^ X) * 16));
  }
  auto read_x = [&](int k0, int n) {
#pragma unroll
    for (int ks = k0; ks < k0 + n; ++ks) xa[ks] = lds16(xb[ks & 7] + (ks >> 3) * 256);
  };
  auto read_w0 = [&]() {
#pragma unroll
    for (int kk = 0; kk < 4; ++kk) wf[0][kk] = trd2(tb + kk * 2048, tb + kk * 2048 + 512);
  };
  f16v acc;
  bool okz = false;
  const u32 vl = ov(lane * 16), vn = ov(f_vn(lane)), trb = ov(L_UP + w * 2048 + f_trb(lane));
  const u32 ib = ov(L_UP + w * 2048 + (lane & 31) * 64 + (((lane >> 5) ^ ((lane >> 1) & 3)) * 16));
  auto block = [&](bool fin, bool first = false, int c0 = 0, int i0 = 0, int nc = 0) {
    __builtin_amdgcn_s_setprio(1);
#pragma unroll
    for (int i = 0; i < 10; ++i) {
      if (i < 4) WAIT_BAR(8); else WAIT_BAR(6);
      const u32 ra = tb + ((i + 1) % 5) * 8192;
      SB();
      acc = mfma32(wf[i & 1][0], xa[4 * i], i == 0 ? zero16() : acc);
      SB();
      wf[(i + 1) & 1][0] = trd2(ra, ra + 512);
      wf[(i + 1) & 1][1] = trd2(ra + 2048, ra + 2048 + 512);
      SB();
      acc = mfma32(wf[i & 1][1], xa[4 * i + 1], acc);
      SB();
      wf[(i + 1) & 1][2] = trd2(ra + 4096, ra + 4096 + 512);
      wf[(i + 1) & 1][3] = trd2(ra + 6144, ra + 6144 + 512);
      SB();
      acc = mfma32(wf[i & 1][2], xa[4 * i + 2], acc);
      SB();
      if (first && i < 9) read_x(4 * i + 4, 4);
      if (i >= i0 && i < i0 + nc) {
        const int u = c0 + i - i0, l = olane();
        const u32 va0 = (16 * wq4 + (l >> 3)) * 4096 + (((l & 7) ^ (l >> 4)) * 16);
        glds2(wsp + (size_t)(64 * rg) * 4096 + (u < 2 ? 4 + u : u - 2) * 128, va0, (va0 ^ 64) + 31744, L_UP + u * 8192 + wq4 * 2048);
      }
      if (i == 5) { pw = pwn; stw = stwn; vo0 = vo0n; vo1 = vo1n; }
      if (i == 6 && fin && wq4 == 0) glds_run<1>(ws + OFF_FLAG + E_Z2 * 1024, vl, L_PL);
      glds2(pw, vo0, vo1, wl + (i % 5) * 8192);
      pw += stw;
      SB();
      acc = mfma32(wf[i & 1][3], xa[4 * i + 3], acc);
      SB();
    }
    __builtin_amdgcn_s_setprio(0);
    asm volatile("s_nop 7\n\ts_nop 3" ::: "memory");
  };
  auto touch_sp = [&](int slot) {
    if ((jb & 3) == slot) touch(wsp, ((b * 4 + wq4) * 64 + olane()) * 128, L_JK);
  };
  auto fill2 = [&]() {
    const u32 so = OFF_FILL + osc(b) * 1024 + w * 128;
    __builtin_amdgcn_raw_buffer_store_b32(vl, wsr, 0, so, 0);
    asm volatile("" ::: "memory");
    __builtin_amdgcn_raw_buffer_store_b32(vl, wsr, 0, so + 64, 0);
    asm volatile("" ::: "memory");
  };
  u32 pk[8];
  v4u o0, o1;
#define SLICE() do { SB(); BAR0(); SB(); } while (0)
#define SLICEW() do { SB(); BAR(); SB(); } while (0)
  float s0, s1, q0, q1;
  auto act = [&](auto kind, auto ec) {
    constexpr int e = ec.value;
    const float a = acc[4 * e], b = acc[4 * e + 1], cc = acc[4 * e + 2], d = acc[4 * e + 3];
    if (kind.value == 0) { pk[2 * e] = pk2(a, b); pk[2 * e + 1] = pk2(cc, d); }
    if (kind.value == 1) phi4(a, b, cc, d, pk[2 * e], pk[2 * e + 1]);
    if (kind.value == 2) gelu4(a, b, cc, d, pk[2 * e], pk[2 * e + 1]);
    if (kind.value == 3) {
      if (e == 0) { s0 = 0.f; s1 = 0.f; q0 = 0.f; q1 = 0.f; }
      gelu4s(a, b, cc, d, pk[2 * e], pk[2 * e + 1], s0, s1, q0, q1);
    }
  };
  std::integral_constant<int, 0> K_NONE, I0; std::integral_constant<int, 1> K_PHI, I1; std::integral_constant<int, 2> K_GELU, I2; std::integral_constant<int, 3> K_Z2, I3;
  auto act4 = [&](auto kind) { SLICE(); act(kind, I0); SLICE(); act(kind, I1); SLICE(); act(kind, I2); SLICE(); act(kind, I3); };
  auto put_sums = [&]() {
    *(LAS f2v*)(L_PART + (w >> 1) * 512 + mh * 256 + (ov(vl) & 496) / 2) = f2v{add32(of(s0 + s1)), add32(of(q0 + q1))};
  };
  auto put_img = [&](u32 off) { swap_tile(pk, o0, o1); sts16(ib + off, o0); sts16((ov(ib) ^ 32) + off, o1); };
  auto get_img = [&]() { o0 = trd2(trb + 32768, trb + 32768 + 256 + 32); o1 = trd2(trb + 32768 + 32, trb + 32768 + 256); };
  auto put_kv = [&](int pc) { st16x2(o0, o1, wsr, vl, 2048, OFF_PKV + (osc(rg) * 160 + 4 * (pc - 20) + mh) * 1024); };
  auto put_q = [&](int pc) { swap_tile(pk, o0, o1); st16x2(o0, o1, wsr, vn, 2048, OFF_PQ + (osc(rg) * 160 + 8 * pc + 2 * mh) * 512); };
  auto put_z1 = [&](int pc) { swap_tile(pk, o0, o1); st16x2(o0, o1, wsr, vn, 2048, OFF_Z1 + (osc(rg) * 216 + 8 * (pc - 60) + 2 * mh) * 512); };
  auto load_kv = [&]() {
    const u8* pk = ws + OFF_PKV + (size_t)(rg * 80 + 5 * cb) * 2048 + (wq4 >> 1) * 81920 + (wq4 & 1) * 5120;
    glds_run<4>(pk, vl, L_JOB + wq4 * 5120); glds_run<1>(pk + 4096, vl, L_JOB + wq4 * 5120 + 4096);
  };
  auto put_kvp = [&](auto&& gap) {
    v4u kv[8];
    const u32 ka = ov(L_JOB + wq4 * 2048 + vl);
#pragma unroll
    for (int k = 0; k < 8; ++k) kv[k] = lds16(ka + (k >> 2) * 10240 + (k & 3) * 1024);
    const f4v z4 = {0.f, 0.f, 0.f, 0.f};
    const v4u one = {0x3f803f80u, 0x3f803f80u, 0x3f803f80u, 0x3f803f80u};
    const u32 so = OFF_KVP + (cb * 18 * 32 + rg) * 1024 + wq4 * 32768;
    const int l = olane(), e16 = 16 * wq4 + (l & 15), d16 = 16 * wq4 + 4 * (l >> 4);
    const int he0 = (e16 * 205) >> 12, he1 = ((e16 + 16) * 205) >> 12, hd0 = (d16 * 205) >> 12, hd1 = ((d16 + 16) * 205) >> 12;
    gap();
    const f4v ta = mfma16(kv[1], kv[7], mfma16(kv[0], kv[6], z4)), tb = mfma16(kv[3], kv[5], mfma16(kv[2], kv[4], z4)),
              tc = mfma16(kv[3], kv[7], mfma16(kv[2], kv[6], z4)), td = mfma16(kv[3], one, mfma16(kv[2], one, z4)),
              te = mfma16(kv[1], kv[5], mfma16(kv[0], kv[4], z4)), tf = mfma16(kv[1], one, mfma16(kv[0], one, z4));
    gap();
    if (hd0 == he1) st16(tag_put(ta, c.t8), wsr, vl, so + 5 * 32768);
    if (hd1 == he0) st16(tag_put(tb, c.t8), wsr, vl, so + 9 * 32768);
    gap();
    if (hd1 == he1) st16(tag_put(tc, c.t8), wsr, vl, so + 32768);
    if ((l & 15) == 0) st16(tag_put(td, c.t8), wsr, vl, so + 14 * 32768);
    gap();
    if (wq4 == 0) {
      st16(tag_put(te, c.t8), wsr, vl, so);
      if ((l & 15) == 0) st16(tag_put(tf, c.t8), wsr, vl, so + 13 * 32768);
    }
  };
  auto kv_fetch = [&](int ev) {
    if (!polled_lds(c, L_PE)) spin_rg(c, ev);
    load_kv();
  };
  auto piece = [&](int r) -> int { return (int)(((u32)osc(blks) >> (8 * r)) & 0xff) * 2 + hf; };
  if (g == 0) {
    read_w0();
    read_x(0, 4);
    fill2();
    STAMP(3);
    {
      {
        block(false, true);
        STAMP(20);
        const int ftb = cb < 7 ? 7 * cb : 48;
        v4u mark = {~0u, ~0u, ~0u, ~0u};
        tie(mark);
        const u32 vh = (ov(vl) & 496) + (ov(vl) & 512) * 8;
        auto put_mark = [&](int ct) { __builtin_amdgcn_raw_buffer_store_b128(mark, wsr, vh, OFF_YO + rg * 47 * 4096 + wq4 * 1024 + ct * 2048 - (ct & 1) * 1536, 16); };
        SLICE(); touch_sp(0); put_mark(5 * cb); put_mark(5 * cb + 1); SB(); act(K_Z2, I0);
        SLICE(); put_mark(5 * cb + 2); put_mark(40 + ftb); SB(); act(K_Z2, I1);
        SLICE(); put_mark(41 + ftb); put_mark(43 + ftb); SB(); act(K_Z2, I2);
        SLICE(); put_mark((cb < 6 ? 44 : 42) + ftb);
        if (w == 0) __builtin_amdgcn_raw_buffer_store_b64(v2u{mark.x, mark.y}, wsr, ov(vl) / 2, OFF_KVF + cb * 16384 + rg * 512, 16);
        SB(); act(K_Z2, I3);
        tie(mark);
        SLICE();
        if (cb + hf > 0) put_img(0); else put_z1(86);
        put_sums();
        SB();
        next_desc(2);
        SLICEW();
        if (cb + hf > 0) fill2();
      }
      {
        block(false);
        STAMP(21);
        SLICE();
        glds_run<2>(ws + OFF_STATS + (osc(rg) * 8 + 2 * wq4) * 1024, vl, L_STAT + osc(wq4) * 2048);
        touch_sp(2);
        act(K_PHI, I0);
        SLICE();
        act(K_PHI, I1); act(K_PHI, I2);
        SLICE();
        act(K_PHI, I3);
        put_img(32768);
        SLICE();
        get_img();
        next_desc(3);
        if (PF_SPA == 2 && (jb & 3) == 2) waitv<1>(); else waitv<0>();
        SLICE();
        put_kv(piece(1));
        const int l = olane();
        const u32 vo = (l >> 4) * 2048 + (16 * wq4 + (l & 15)) * 16;
        v4u v0 = lds16(L_STAT + vo), v1 = lds16(L_STAT + vo + 1024);
        SLICE();
        auto stale = [&]() -> bool {
          u32 bad = ((v0.y ^ c.tag) | (v0.w ^ c.tag)) | ((v1.y ^ c.tag) | (v1.w ^ c.tag));
          asm volatile("" : "+v"(bad));
          return __builtin_amdgcn_ballot_w64(bad != 0) != 0;
        };
        if (stale()) {
          SLOW(11);
          NOUNROLL for (int sp = 0; sp < SPIN; ++sp) {
            v0 = __builtin_amdgcn_raw_buffer_load_b128(wsr, vo, OFF_STATS + rg * 8192, 16);
            v1 = __builtin_amdgcn_raw_buffer_load_b128(wsr, vo, OFF_STATS + rg * 8192 + 1024, 16);
            if (!stale()) break;
            __builtin_amdgcn_s_sleep(1);
          }
        }
        const float mean = of(add32(add16(of(asf(v0.x) + asf(v1.x)))) * (1.0f / 864.0f));
        const float msq = of(add32(add16(of(asf(v0.z) + asf(v1.z)))) * (1.0f / 864.0f));
        const float rs = __builtin_amdgcn_rsqf(fmaxf(msq - mean * mean, 0.f) + 1e-5f);
        const u32 na = ov(L_NRM + (16 * wq4 + (l & 15)) * 4);
        *(LAS float*)(na) = rs;
        *(LAS float*)(na + 256) = -mean * rs;
      }
      {
        block(false);
        STAMP(22);
        SLICE();
        if (w == 0) post(c, E_Z2);
        if (cb < 2) {
          act(K_NONE, I0); act(K_NONE, I1); act(K_NONE, I2); act(K_NONE, I3);
          put_img(32768);
          SLICE();
          get_img();
          SLICE();
          put_kv(piece(2));
        } else {
          act(K_PHI, I0);
          SLICE();
          act(K_PHI, I1); act(K_PHI, I2);
          SLICE();
          act(K_PHI, I3);
          put_q(piece(2));
        }
        SLICE();
        next_desc(4);
        SLICE();
        SLICE();
        if (cb < 2) waitv<0>();
      }
      KEEP();
      block(late, false, 4, 0, 2);
      MARK();
      STAMP(23);
    }
    if (!late) {
      if (w == 0) poll_lds(c, E_KV, L_PE);
      SLICE(); act(K_GELU, I0);
      SLICE(); act(K_GELU, I1);
      if (w == 0) waitv<0>();
      SLICE(); kv_fetch(E_KV); act(K_GELU, I2);
      SLICE(); act(K_GELU, I3);
      SLICE();
      put_z1(piece(3));
      SB(); WAIT_BAR(2); SB();
      put_kvp([&]() {});
    }
  } else {
    {
      {
        prefill(0, 1);
        BAR0();
        prefill(1, 2);
        read_x(0, 10);
        BAR0();
        prefill(2, 3);
        read_x(10, 10);
        BAR0();
        prefill(3, 4);
        read_x(20, 10);
        BAR0();
        prefill(4, 5);
        read_x(30, 10);
        waitv<8>();
        BAR0();
        read_w0();
        fill2();
        BAR0();
      }
      {
        block(false);
        SLICE();
        touch_sp(1);
        if (cb < 6) {
          act(K_Z2, I0); SLICE(); act(K_Z2, I1); SLICE(); act(K_Z2, I2); SLICE(); act(K_Z2, I3);
          SLICE();
          put_img(0);
          put_sums();
        } else {
          act(K_PHI, I0);
          SLICE();
          act(K_PHI, I1); act(K_PHI, I2);
          SLICE();
          act(K_PHI, I3);
          put_img(32768);
          SLICE();
          get_img();
          SLICE();
          put_kv(piece(0));
        }
        SB();
        next_desc(2);
        SLICEW();
        const u32 a = ov(L_PART + ov(vl) / 2);
        const f2v s0 = *(const LAS f2v*)(a), s1 = *(const LAS f2v*)(a + 512), s2 = *(const LAS f2v*)(a + 1024), s3 = *(const LAS f2v*)(a + 1536);
        SB();
        {
          const int bb = b < 235 ? osc(b) : 0, ks = (bb * 205) >> 10, ff = bb - ks * 5, l = olane();
          const u8* src = (ks < 20 ? wo : wout - 20 * 32 * 1280) + (size_t)ks * 32 * 1280 + ff * 256;
          const u32 vo = (8 * wq4 + (l >> 4)) * 1280 + (l & 15) * 16;
          glds2(src, vo, vo + 4 * 1280 - 1024, L_FL + osc(wq4) * 2048);
        }
        SB();
        if (w == 4) {
          const f2v z = {0.f, 0.f};
          const f2v sa_ = cb > 0 ? s0 : z, sb_ = cb < 6 ? s2 : z, sc_ = cb < 6 ? s3 : z;
          const f2v s = {of(of(s1.x + sa_.x) + of(sb_.x + sc_.x)), of(of(s1.y + sa_.y) + of(sb_.y + sc_.y))};
          const u32 tg = osc(c.tag);
          st16(v4u{asu(s.x), tg, asu(s.y), tg}, wsr, vl, OFF_STATS + (osc(rg) * 8 + cb) * 1024);
        }
      }
      {
        block(false);
        SLICE();
        touch_sp(3);
        const u32 na = ov(L_NRM + mh * 128 + (ov(vl) >> 8) * 32), t = ov(trb - 8192);
        const f4v c0 = ldsf4(na), c1 = ldsf4(na + 16), h0 = ldsf4(na + 256), h1 = ldsf4(na + 272);
        v4u f0 = trd2(t, t + 256 + 32), f1 = trd2(t + 32, t + 256);
        SB();
        act(K_NONE, I0); act(K_NONE, I1); act(K_NONE, I2); act(K_NONE, I3);
        put_img(32768);
        SLICE();
        const u32 so = OFF_Z2T + ((2 * osc(rg) + mh) * 54 + 4 * cb + 2 * hf - 2) * 1024;
        v4u n0 = norm8(f0, c0, c1, h0, h1), n1 = norm8(f1, c0, c1, h0, h1);
        f0 = trd2(trb, trb + 256 + 32); f1 = trd2(trb + 32, trb + 256);
        SLICE();
        if (cb + hf > 0) st16x2(n0, n1, wsr, vl, 1024, so);
        n0 = norm8(f0, c0, c1, h0, h1);
        get_img();
        SLICE();
        n1 = norm8(f1, c0, c1, h0, h1);
        if (cb < 6) st16x2(n0, n1, wsr, vl, 1024, so + 32 * 1024);
        const int l = olane();
        const u32 a = ov(L_FL + (8 * (l >> 4) + ((l & 15) >> 2)) * 256 + wq4 * 64 + (l & 3) * 8);
        const v4u t0 = trd2(a, a + 1024), t1 = trd2(a + 32, a + 1024 + 32);
        SLICE();
        put_kv(piece(1));
        next_desc(3);
        SLICE();
        if (b < 235) {
          const int bb = osc(b), ks = (bb * 205) >> 10, ff = bb - ks * 5;
          const int ct = ff * 8 + 2 * wq4, cq0 = (ct * 205) >> 10, cq1 = ((ct + 1) * 205) >> 10;
          st16(t0, wsr, vl, OFF_W3T + (cq0 * 230 + ks * 5 + ct) * 1024);
          st16(t1, wsr, vl, OFF_W3T + (cq1 * 230 + ks * 5 + ct + 1) * 1024);
        }
      }
      {
        block(false, false, 0, 4, 4);
        if (w == 4) poll_lds(c, E_Z2, L_PE);
        SLICE();
        if (w == 4) post(c, E_KV);
        if (cb < 4) {
          act(K_PHI, I0);
          SLICE();
          act(K_PHI, I1); act(K_PHI, I2);
          if (w == 4) waitv<1>();
          SLICE();
          kv_fetch(E_Z2);
          SLICE();
          act(K_PHI, I3);
          put_q(piece(2));
          SLICE();
        } else {
          act(K_GELU, I0);
          SLICE(); act(K_GELU, I1);
          if (w == 4) waitv<1>();
          SLICE();
          if (cb < 6) kv_fetch(E_Z2);
          act(K_GELU, I2);
          SLICE(); act(K_GELU, I3);
          SLICE();
          if (cb == 6 && w == 4) poll_lds(c, E_KV, L_PE);
          put_z1(piece(2));
        }
        SB(); WAIT_BAR(2); SB();
      }
    }
    if (!late) {
      KEEP(); MARK();
      if (cb < 6) put_kvp([&]() { SLICEW(); });
      else {
        kv_fetch(E_KV);
        BAR0(); BAR0(); BAR0();
        WAIT_BAR(0);
        put_kvp([&]() {});
      }
      MARK();
#pragma unroll
      for (int k = 0; k < 40; ++k) asm volatile("" : "=v"(xa[k]));
#pragma unroll
      for (int k = 0; k < 4; ++k) asm volatile("" : "=v"(wf[0][k]));
    }
    if (osc(cb) == 7) {
      next_desc(4);
      block(true);
    }
  }
  okz = all_eq(lds16(L_PL + vl), c.tag);
  STAMP(4); MARK();
  if (!okz) spin_all(c, E_Z2);
  STAMP(5); MARK();
  const u32 so_last = OFF_Z1 + (rg * 216 + 8 * ((int)(blks >> 24) * 2 + hf - 60) + 2 * mh) * 512;
  v4u ya[12][2];
  if (g == 1) phase2<3, true>(c, late, acc, so_last, epoch, ya DBG_PASS);
  else if (cb < 6) phase2<4, false>(c, late, acc, so_last, epoch, ya DBG_PASS);
  else phase2<3, false>(c, late, acc, so_last, epoch, ya DBG_PASS);
  phase3(c, ya DBG_PASS);
  asm volatile(".Lk_end:");
}

extern "C" void kernel_launch(void* const* d_in, const int* in_sizes, int n_in, void* d_out, int out_size, void* d_ws, size_t ws_size,
                              hipStream_t stream) {
  (void)in_sizes; (void)n_in; (void)out_size; (void)ws_size;
  (void)hipFuncSetAttribute((const void*)fused, hipFuncAttributeMaxDynamicSharedMemorySize, LDS_BYTES);
  fused<<<256, 512, LDS_BYTES, stream>>>((const u8*)d_in[0], (const u8*)d_in[1], (const u8*)d_in[2], (const u8*)d_in[4], (const u8*)d_in[5],
                                         (const u8*)d_in[6], (const u8*)d_in[7], (u8*)d_out, (u8*)d_ws);
}
```
